```python
import torch

def _softmax(x, axis=-1):
    return torch.softmax(x, dim=axis)

def _gelu(x):
    return torch.nn.functional.gelu(x, approximate='tanh')

def _silu(x):
    return torch.nn.functional.silu(x)

def _sigmoid(x):
    return torch.sigmoid(x)

def _layernorm(x, axis=-1, eps=1e-05):
    m = x.mean(dim=axis, keepdim=True)
    v = x.var(dim=axis, keepdim=True, unbiased=False)
    return (x - m) / torch.sqrt(v + eps)

def _scatter_add(tgt, idx, src, axis=0):
    return tgt.clone().index_add_(axis, idx, src)

def _segment_sum(src, seg, n):
    out = torch.zeros((n,) + src.shape[1:], dtype=src.dtype)
    return out.index_add_(0, seg, src)

def _pad(x, pw):
    flat = tuple((v for pair in reversed(pw) for v in pair))
    return torch.nn.functional.pad(x, flat)
kernel_name = 'adaLN_cond__mha_block_cached__geglu_ffn__decode'

def setup_inputs():

    _w = lambda a: (lambda t: t.to(torch.bfloat16) if t.is_floating_point() else t)(torch.as_tensor(a))

    def _A_setup():
        B, Sq, D = (2, 1, 448)
        H, Sk, Dh = (16, 4096, 256)
        return {'x': _w((lambda *s: torch.randn(*s, dtype=torch.bfloat16) * 0.1)(B, Sq, D)), 'k_cache': _w((lambda *s: torch.randn(*s, dtype=torch.bfloat16) * 0.8)(B, H, Sk, Dh)), 'v_cache': _w((lambda *s: torch.randn(*s, dtype=torch.bfloat16) * 0.8)(B, H, Sk, Dh)), 'wq': _w((lambda *s: torch.randn(*s, dtype=torch.bfloat16) * 0.8)(D, H * Dh) * D ** (-0.5)), 'wo': _w((lambda *s: torch.randn(*s, dtype=torch.bfloat16) * 0.8)(H * Dh, D) * (H * Dh) ** (-0.5))}
    _A_in = _A_setup()

    def _B_setup():
        B, S, D, F = (2, 1, 448, 1792)
        return {'x': _w((lambda *s: torch.randn(*s, dtype=torch.bfloat16) * 0.1)(B, S, D)), 'w_gate': _w((lambda *s: torch.randn(*s, dtype=torch.bfloat16) * 0.1)(D, F)), 'w_up': _w((lambda *s: torch.randn(*s, dtype=torch.bfloat16) * 0.1)(D, F)), 'w_down': _w((lambda *s: torch.randn(*s, dtype=torch.bfloat16) * 0.1)(F, D))}
    _B_in = _B_setup()
    return {'A_x': _A_in['x'], 'A_k_cache': _A_in['k_cache'], 'A_v_cache': _A_in['v_cache'], 'A_wq': _A_in['wq'], 'A_wo': _A_in['wo'], 'B_x': _B_in['x'], 'B_w_gate': _B_in['w_gate'], 'B_w_up': _B_in['w_up'], 'B_w_down': _B_in['w_down'], 'c': _w((lambda *s: torch.randn(*s, dtype=torch.bfloat16) * 0.8)(2, 448) * 8.0), 'w_mod': _w((lambda *s: torch.randn(*s, dtype=torch.bfloat16) * 0.8)(448, 6 * 448) * 448 ** (-0.5) * 8.0)}

def reference(A_x, A_k_cache, A_v_cache, A_wq, A_wo, B_x, B_w_gate, B_w_up, B_w_down, c, w_mod):

    def _A_ref(x, k_cache, v_cache, wq, wo):
        B, Sq, D = x.shape
        H, Dh = (16, 256)
        q = (lambda x, *d: x.permute(*d))((x @ wq).reshape(B, Sq, H, Dh), 0, 2, 1, 3)
        scores = torch.einsum('bhqd,bhkd->bhqk', q, k_cache) * Dh ** (-0.5)
        p = _softmax(scores, axis=-1)
        o = torch.einsum('bhqk,bhkd->bhqd', p, v_cache)
        return (lambda x, *d: x.permute(*d))(o, 0, 2, 1, 3).reshape(B, Sq, H * Dh) @ wo

    def _B_ref(x, w_gate, w_up, w_down):
        return _gelu(x @ w_gate) * (x @ w_up) @ w_down
    x = A_x
    mod = (_silu(c) @ w_mod).reshape(c.shape[0], 6, 1, -1)
    sa, ca, ga, sb, cb, gb = (mod[:, 0].clone(), mod[:, 1].clone(), mod[:, 2].clone(), mod[:, 3].clone(), mod[:, 4].clone(), mod[:, 5].clone())
    h = _layernorm(x, axis=-1) * (1 + ca) + sa
    x = x + ga * _A_ref(h, A_k_cache, A_v_cache, A_wq, A_wo)
    h = _layernorm(x, axis=-1) * (1 + cb) + sb
    x = x + gb * _B_ref(h, B_w_gate, B_w_up, B_w_down)
    return x
```

```cpp
#include <hip/hip_runtime.h>
#include <hip/hip_bf16.h>
#include <stdint.h>

typedef unsigned short u16;
typedef unsigned v4u __attribute__((ext_vector_type(4)));
typedef __bf16 bf16x2 __attribute__((ext_vector_type(2)));

constexpr int D = 448, H = 16, SK = 4096, DH = 256, F = 1792, NB = 2;

__device__ __forceinline__ float bf2f(u16 b) { return __uint_as_float((unsigned)b << 16); }
__device__ __forceinline__ float rbf(float f) {
  unsigned u = __float_as_uint(f); u += 0x7fffu + ((u >> 16) & 1u); return __uint_as_float(u & 0xffff0000u); }
__device__ __forceinline__ u16 f2bf(float f) { unsigned u = __float_as_uint(f); u += 0x7fffu + ((u >> 16) & 1u); return (u16)(u >> 16); }
__device__ __forceinline__ float lo(unsigned w) { return __uint_as_float(w << 16); }
__device__ __forceinline__ float hi(unsigned w) { return __uint_as_float(w & 0xffff0000u); }
__device__ __forceinline__ float wsum(float v) { for (int o = 32; o > 0; o >>= 1) v += __shfl_xor(v, o); return v; }
__device__ __forceinline__ float wmax(float v) { for (int o = 32; o > 0; o >>= 1) v = fmaxf(v, __shfl_xor(v, o)); return v; }

template <int K>
__device__ __forceinline__ void gemv8(const float* in0, const float* in1, const u16* W, int ldw, int n0, float (&r)[16]) {
  const int lane = threadIdx.x & 63;
  float a[16];
#pragma unroll
  for (int j = 0; j < 16; ++j) a[j] = 0.f;
  for (int k = lane; k < K; k += 64) {
    v4u w = *(const v4u*)(W + (size_t)k * ldw + n0);
    float x0 = in0[k], x1 = in1[k];
#pragma unroll
    for (int j = 0; j < 4; ++j) {
      a[2 * j] += x0 * lo(w[j]); a[2 * j + 1] += x0 * hi(w[j]);
      a[8 + 2 * j] += x1 * lo(w[j]); a[8 + 2 * j + 1] += x1 * hi(w[j]);
    }
  }
#pragma unroll
  for (int j = 0; j < 16; ++j) r[j] = wsum(a[j]);
}

__global__ void __launch_bounds__(64) k_mod(const u16* c, const u16* wmod, float* mod) {
  __shared__ float s[2][D];
  for (int i = threadIdx.x; i < 2 * D; i += 64) { float v = bf2f(c[i]); s[i / D][i % D] = rbf(v / (1.f + __expf(-v))); }
  __syncthreads();
  float r[16];
  gemv8<D>(s[0], s[1], wmod, 6 * D, blockIdx.x * 8, r);
  if (threadIdx.x == 0) for (int j = 0; j < 8; ++j) { mod[blockIdx.x * 8 + j] = rbf(r[j]); mod[6 * D + blockIdx.x * 8 + j] = rbf(r[8 + j]); }
}

__device__ __forceinline__ void ln_mod(const float* x, const float* sh, const float* sc, float* h) {
  const int lane = threadIdx.x & 63;
  float s = 0.f;
  for (int k = lane; k < D; k += 64) s += x[k];
  const float mean = wsum(s) * (1.f / D);
  float v = 0.f;
  for (int k = lane; k < D; k += 64) { float d = x[k] - mean; v += d * d; }
  const float var = rbf(wsum(v) * (1.f / D));
  const float mb = rbf(mean);
  const float sd = rbf(sqrtf(rbf(var + 1e-5f)));
  for (int k = lane; k < D; k += 64) {
    float n = rbf(rbf(x[k] - mb) / sd);
    h[k] = rbf(rbf(n * rbf(1.f + sc[k])) + sh[k]);
  }
}

__global__ void __launch_bounds__(64) k_q(const u16* x, const float* mod, const u16* wq, float* q) {
  __shared__ float xs[2][D], hs[2][D];
  for (int i = threadIdx.x; i < 2 * D; i += 64) xs[i / D][i % D] = bf2f(x[i]);
  __syncthreads();
  for (int b = 0; b < 2; ++b) ln_mod(xs[b], mod + b * 6 * D, mod + b * 6 * D + D, hs[b]);
  __syncthreads();
  float r[16];
  gemv8<D>(hs[0], hs[1], wq, H * DH, blockIdx.x * 8, r);
  if (threadIdx.x == 0) for (int j = 0; j < 8; ++j) { q[blockIdx.x * 8 + j] = rbf(r[j]); q[H * DH + blockIdx.x * 8 + j] = rbf(r[8 + j]); }
}

__global__ void __launch_bounds__(512) k_scores(const float* q, const u16* kc, float* sc) {
  const int bh = blockIdx.x, sp = blockIdx.y, w = threadIdx.x >> 6, lane = threadIdx.x & 63;
  const int l32 = lane & 31, half = lane >> 5;
  float qv[8];
  for (int j = 0; j < 8; ++j) qv[j] = q[bh * DH + l32 * 8 + j];
  const u16* kb = kc + ((size_t)bh * SK + sp * 512 + w * 64) * DH;
  for (int i = 0; i < 32; ++i) {
    v4u kv = *(const v4u*)(kb + (size_t)(2 * i + half) * DH + l32 * 8);
    float a = 0.f;
#pragma unroll
    for (int j = 0; j < 4; ++j) { a += qv[2 * j] * lo(kv[j]); a += qv[2 * j + 1] * hi(kv[j]); }
    for (int o = 16; o > 0; o >>= 1) a += __shfl_xor(a, o);
    if (l32 == 0) sc[(size_t)bh * SK + sp * 512 + w * 64 + 2 * i + half] = rbf(a) * 0.0625f;
  }
}

__global__ void __launch_bounds__(512) k_pv(const float* sc, const u16* vc, float* opart) {
  __shared__ float red[8]; __shared__ float p[512]; __shared__ float acc_s[8][2][256];
  const int bh = blockIdx.x, sp = blockIdx.y, w = threadIdx.x >> 6, lane = threadIdx.x & 63;
  float s[8]; float m = -INFINITY;
  for (int j = 0; j < 8; ++j) { s[j] = sc[(size_t)bh * SK + j * 512 + threadIdx.x]; m = fmaxf(m, s[j]); }
  m = wmax(m); if (lane == 0) red[w] = m; __syncthreads();
  m = red[0]; for (int j = 1; j < 8; ++j) m = fmaxf(m, red[j]);
  __syncthreads();
  float l = 0.f; for (int j = 0; j < 8; ++j) l += __expf(s[j] - m);
  l = wsum(l); if (lane == 0) red[w] = l; __syncthreads();
  l = 0.f; for (int j = 0; j < 8; ++j) l += red[j];
  float mine = 0.f; for (int j = 0; j < 8; ++j) if (j == sp) mine = s[j];
  p[threadIdx.x] = rbf(__expf(mine - m) / l);
  __syncthreads();
  const int l32 = lane & 31, half = lane >> 5;
  const u16* vb = vc + ((size_t)bh * SK + sp * 512 + w * 64) * DH;
  float a[8]; for (int j = 0; j < 8; ++j) a[j] = 0.f;
  for (int i = 0; i < 32; ++i) {
    v4u vv = *(const v4u*)(vb + (size_t)(2 * i + half) * DH + l32 * 8);
    float pp = p[w * 64 + 2 * i + half];
#pragma unroll
    for (int j = 0; j < 4; ++j) { a[2 * j] += pp * lo(vv[j]); a[2 * j + 1] += pp * hi(vv[j]); }
  }
  for (int j = 0; j < 8; ++j) acc_s[w][half][l32 * 8 + j] = a[j];
  __syncthreads();
  if (threadIdx.x < 256) { float t = 0.f; for (int ww = 0; ww < 8; ++ww) t += acc_s[ww][0][threadIdx.x] + acc_s[ww][1][threadIdx.x];
    opart[((size_t)bh * 8 + sp) * DH + threadIdx.x] = t; }
}

__global__ void __launch_bounds__(256) k_wo(const float* opart, const u16* wo, const u16* x, const float* mod, float* x2) {
  __shared__ float o[2][H * DH]; __shared__ float red[4][16];
  for (int i = threadIdx.x; i < 2 * H * DH; i += 256) { int bh = i / DH, d = i % DH; float t = 0.f; for (int s = 0; s < 8; ++s) t += opart[((size_t)bh * 8 + s) * DH + d]; o[i / (H * DH)][i % (H * DH)] = rbf(t); }
  __syncthreads();
  const int w = threadIdx.x >> 6;
  float r[16];
  gemv8<1024>(o[0] + w * 1024, o[1] + w * 1024, wo + (size_t)w * 1024 * D, D, blockIdx.x * 8, r);
  if ((threadIdx.x & 63) == 0) for (int j = 0; j < 16; ++j) red[w][j] = r[j];
  __syncthreads();
  if (threadIdx.x < 16) { int b = threadIdx.x >> 3, n = blockIdx.x * 8 + (threadIdx.x & 7);
    float t = rbf(red[0][threadIdx.x] + red[1][threadIdx.x] + red[2][threadIdx.x] + red[3][threadIdx.x]);
    x2[b * D + n] = rbf(bf2f(x[b * D + n]) + rbf(mod[b * 6 * D + 2 * D + n] * t)); }
}

__global__ void __launch_bounds__(64) k_gu(const float* x2, const float* mod, const u16* wg, const u16* wu, float* act) {
  __shared__ float xs[2][D], hs[2][D];
  for (int i = threadIdx.x; i < 2 * D; i += 64) xs[i / D][i % D] = x2[i];
  __syncthreads();
  for (int b = 0; b < 2; ++b) ln_mod(xs[b], mod + b * 6 * D + 3 * D, mod + b * 6 * D + 4 * D, hs[b]);
  __syncthreads();
  float g[16], u[16];
  gemv8<D>(hs[0], hs[1], wg, F, blockIdx.x * 8, g);
  gemv8<D>(hs[0], hs[1], wu, F, blockIdx.x * 8, u);
  if (threadIdx.x == 0) for (int j = 0; j < 16; ++j) {
    float gg = rbf(g[j]), uu = rbf(u[j]);
    float t = tanhf(0.7978845608028654f * (gg + 0.044715f * gg * gg * gg));
    float ge = rbf(0.5f * gg * (1.f + t));
    act[(j >> 3) * F + blockIdx.x * 8 + (j & 7)] = rbf(ge * uu);
  }
}

__global__ void __launch_bounds__(64) k_down(const float* act, const u16* wd, const float* x2, const float* mod, u16* out) {
  float r[16];
  gemv8<F>(act, act + F, wd, D, blockIdx.x * 8, r);
  if (threadIdx.x == 0) for (int j = 0; j < 16; ++j) { int b = j >> 3, n = blockIdx.x * 8 + (j & 7);
    out[b * D + n] = f2bf(x2[b * D + n] + rbf(mod[b * 6 * D + 5 * D + n] * rbf(r[j]))); }
}

extern "C" void kernel_launch(void* const* d_in, const int* in_sizes, int n_in, void* d_out, int out_size, void* d_ws, size_t ws_size, hipStream_t stream) {
  const u16* x = (const u16*)d_in[0]; const u16* kc = (const u16*)d_in[1]; const u16* vc = (const u16*)d_in[2];
  const u16* wq = (const u16*)d_in[3]; const u16* wo = (const u16*)d_in[4];
  const u16* wg = (const u16*)d_in[6]; const u16* wu = (const u16*)d_in[7]; const u16* wd = (const u16*)d_in[8];
  const u16* c = (const u16*)d_in[9]; const u16* wmod = (const u16*)d_in[10];
  float* ws = (float*)d_ws;
  float* mod = ws; float* q = ws + 8192; float* sc = ws + 32768; float* opart = sc + 32 * SK; float* x2 = opart + 32 * 8 * DH; float* act = x2 + 1024;
  k_mod<<<336, 64, 0, stream>>>(c, wmod, mod);
  k_q<<<512, 64, 0, stream>>>(x, mod, wq, q);
  k_scores<<<dim3(32, 8), 512, 0, stream>>>(q, kc, sc);
  k_pv<<<dim3(32, 8), 512, 0, stream>>>(sc, vc, opart);
  k_wo<<<56, 256, 0, stream>>>(opart, wo, x, mod, x2);
  k_gu<<<224, 64, 0, stream>>>(x2, mod, wg, wu, act);
  k_down<<<56, 64, 0, stream>>>(act, wd, x2, mod, (u16*)d_out);
}
```

```cpp
#include <hip/hip_runtime.h>
#include <stdint.h>

typedef unsigned short u16;
typedef unsigned v4u __attribute__((ext_vector_type(4)));
typedef unsigned v2u __attribute__((ext_vector_type(2)));
typedef float v4f __attribute__((ext_vector_type(4)));
typedef float v2f __attribute__((ext_vector_type(2)));
typedef short v8s __attribute__((ext_vector_type(8)));
typedef short v4s __attribute__((ext_vector_type(4)));
typedef __bf16 bf16x2 __attribute__((ext_vector_type(2)));
#define GAS __attribute__((address_space(1)))
#define LAS __attribute__((address_space(3)))

#define KDEPTH 4
#define VDEPTH 4
#define WDEPTH 4
#define PSTART 20
#define PSLP 8
#define POL_W 2
#define DBG_STAMP 0

namespace {
constexpr int D = 448, NH = 16, SK = 4096, DH = 256, F = 1792;
constexpr int NTHR = 768;
constexpr int NF = 56;
constexpr int AUX_NT = 2, AUX_SC1 = 16;
constexpr unsigned SPIN_CAP = 1u << 16;

constexpr int WS_EPOCH = 0;
constexpr int WS_MOD = 256;
constexpr int WS_X2 = 22528;
constexpr int WS_ST = 26624;
constexpr int WS_Q = 32768;
constexpr int WS_SC = WS_Q + 32 * 8192;
constexpr int WS_P1 = WS_SC + 32 * 16384;
constexpr int WS_P2 = WS_P1 + 2 * 56 * 8192;
constexpr int WS_END = WS_P2 + 2 * 56 * NF * 64;
constexpr int WS_STAMP = 4 << 20;
static_assert(WS_END <= WS_STAMP, "ws map");

constexpr int L_WO = 0;
constexpr int L_GU = 57344;
constexpr int L_DN = L_GU + 57344;
constexpr int L_Q = L_DN + 28672;
constexpr int L_H1 = L_Q + 512;
constexpr int L_P = L_H1 + 512;
constexpr int L_OA = L_P + 4096;
constexpr int L_H2 = L_OA + 2304;
constexpr int L_WM = L_GU;
constexpr int L_WQ = L_DN;
constexpr int L_ACT = L_H2 + 1792;
constexpr int L_FLAG = L_ACT + 128;
constexpr int L_END = L_FLAG + 64;
static_assert(L_END <= 160 * 1024 && L_END > 80 * 1024, "lds");
enum { F_Q = 0, F_P = 1, F_WO = 2, F_SC = 3, F_MOD = 4, F_WQ = 5 };

struct Params {
  const u16 *x, *kc, *vc, *wq, *wo, *wg, *wu, *wd, *c, *wmod;
  u16* out;
  unsigned char* ws;
};

__device__ __forceinline__ float lo16(unsigned w) { return __uint_as_float(w << 16); }
__device__ __forceinline__ float hi16(unsigned w) { return __uint_as_float(w & 0xffff0000u); }
__device__ __forceinline__ unsigned pack2(float a, float b) {
  bf16x2 v = {(__bf16)a, (__bf16)b};
  return __builtin_bit_cast(unsigned, v);
}
__device__ __forceinline__ float rbf(float f) { return lo16(pack2(f, f)); }
__device__ __forceinline__ float dot2(unsigned a, unsigned b, float c) {
  return __builtin_amdgcn_fdot2_f32_bf16(__builtin_bit_cast(bf16x2, a), __builtin_bit_cast(bf16x2, b), c, false);
}
__device__ __forceinline__ float rcp_nr(float x) { const float r = __builtin_amdgcn_rcpf(x); return fmaf(fmaf(-x, r, 1.f), r, r); }
template <int N> __device__ __forceinline__ void waitvm() {
  asm volatile("" ::: "memory");
  __builtin_amdgcn_s_waitcnt((N & 15) | ((N >> 4) << 14) | 0x0F70);
  asm volatile("" ::: "memory");
}
__device__ __forceinline__ void waitlds() { asm volatile("s_waitcnt lgkmcnt(0)" ::: "memory"); }
__device__ __forceinline__ void bar() {
  __builtin_amdgcn_sched_barrier(0);
  asm volatile("s_waitcnt lgkmcnt(0)" ::: "memory");
  __builtin_amdgcn_s_barrier();
  asm volatile("" ::: "memory");
  __builtin_amdgcn_sched_barrier(0);
}
template <int CTRL> __device__ __forceinline__ float dpp(float x) {
  return __builtin_bit_cast(float, __builtin_amdgcn_mov_dpp(__builtin_bit_cast(int, x), CTRL, 0xf, 0xf, true));
}
template <int CTRL> __device__ __forceinline__ unsigned dppu(unsigned x) {
  return (unsigned)__builtin_amdgcn_mov_dpp((int)x, CTRL, 0xf, 0xf, true);
}
constexpr int XOR1 = 0xB1, XOR2 = 0x4E, XOR7 = 0x141, ROR8 = 0x128, ROR4 = 0x124;
__device__ __forceinline__ float tr32(float a, float b) {
  auto r = __builtin_amdgcn_permlane32_swap(__float_as_uint(a), __float_as_uint(b), false, false);
  return __uint_as_float(r[0]) + __uint_as_float(r[1]);
}
__device__ __forceinline__ float tr16(float a, float b) {
  auto r = __builtin_amdgcn_permlane16_swap(__float_as_uint(a), __float_as_uint(b), false, false);
  return __uint_as_float(r[0]) + __uint_as_float(r[1]);
}
template <int CTRL> __device__ __forceinline__ float trd(float a, float b, bool bit) {
  const float t1 = a + dpp<CTRL>(a), t2 = b + dpp<CTRL>(b);
  return bit ? t2 : t1;
}
__device__ __forceinline__ float sum32(float a) { return tr32(a, a); }
__device__ __forceinline__ float sum16(float a) { return tr16(a, a); }
__device__ __forceinline__ float wave_sum(float v) {
  v += dpp<XOR1>(v); v += dpp<XOR2>(v); v += dpp<XOR7>(v); v += dpp<ROR8>(v);
  v = sum16(v); v = sum32(v);
  return v;
}
__device__ __forceinline__ float wave_max(float v) {
  v = fmaxf(v, dpp<XOR1>(v)); v = fmaxf(v, dpp<XOR2>(v)); v = fmaxf(v, dpp<XOR7>(v)); v = fmaxf(v, dpp<ROR8>(v));
  auto r = __builtin_amdgcn_permlane16_swap(__float_as_uint(v), __float_as_uint(v), false, false);
  v = fmaxf(__uint_as_float(r[0]), __uint_as_float(r[1]));
  auto t = __builtin_amdgcn_permlane32_swap(__float_as_uint(v), __float_as_uint(v), false, false);
  return fmaxf(__uint_as_float(t[0]), __uint_as_float(t[1]));
}
__device__ __forceinline__ v4f mfma(v4u a, v4u b, v4f c) {
  return __builtin_amdgcn_mfma_f32_16x16x32_bf16(__builtin_bit_cast(v8s, a), __builtin_bit_cast(v8s, b), c, 0, 0, 0);
}
__device__ __forceinline__ v4u bfrag(LAS unsigned char* q, int stride) {
  const v2u a = __builtin_bit_cast(v2u, __builtin_amdgcn_ds_read_tr16_b64_v4i16((LAS v4s*)q));
  const v2u b = __builtin_bit_cast(v2u, __builtin_amdgcn_ds_read_tr16_b64_v4i16((LAS v4s*)(q + 4 * stride)));
  return v4u{a.x, a.y, b.x, b.y};
}

typedef __amdgpu_buffer_rsrc_t rsrc_t;
__device__ __forceinline__ rsrc_t mkrsrc(const void* p, int bytes) { return __builtin_amdgcn_make_buffer_rsrc((void*)p, 0, bytes, 0x00020000); }
__device__ __forceinline__ void publish(rsrc_t rs, int off, unsigned a, unsigned b, unsigned T) {
  v4u v = {a, T, b, T};
  __builtin_amdgcn_raw_buffer_store_b128(v, rs, off, 0, AUX_SC1);
}
template <int N, int SLP> __device__ __forceinline__ void gather(rsrc_t rs, const int (&off)[N], v4u (&v)[N], unsigned T) {
  for (unsigned spins = 0; spins < SPIN_CAP; ++spins) {
    asm volatile("" ::: "memory");
    bool ok = true;
#pragma unroll
    for (int k = 0; k < N; ++k) v[k] = __builtin_amdgcn_raw_buffer_load_b128(rs, off[k], 0, AUX_SC1);
#pragma unroll
    for (int k = 0; k < N; ++k) ok &= (v[k].y == T) & (v[k].w == T);
    if (__all(ok)) break;
    __builtin_amdgcn_s_sleep(SLP);
  }
}
__device__ __forceinline__ void lds_wait(volatile LAS unsigned* f, unsigned want) {
  for (unsigned spins = 0; spins < (1u << 22); ++spins) {
    if (__builtin_amdgcn_readfirstlane(*f) >= want) break;
    __builtin_amdgcn_s_sleep(1);
  }
  asm volatile("" ::: "memory");
}
__device__ __forceinline__ void lds_inc(volatile LAS unsigned* f) {
  __hip_atomic_fetch_add((LAS unsigned*)f, 1u, __ATOMIC_RELAXED, __HIP_MEMORY_SCOPE_WORKGROUP);
}
constexpr int pairs_done(int i) {
  if (i < PSTART) return 0;
  int a = (i - VDEPTH + 1) / 2, b = (i - PSTART + 1) * 3;
  return a < b ? a : b;
}
__device__ __forceinline__ float ln_rsd(float var) { return rcp_nr(rbf(__builtin_amdgcn_sqrtf(rbf(rbf(var) + 1e-5f)))); }

#define STAMP(i)
#define STAMP_FLUSH()

__global__ void __launch_bounds__(NTHR, 3) fused_block(Params p) {
  extern __shared__ __attribute__((aligned(16))) unsigned char smem[];
  LAS unsigned char* L = (LAS unsigned char*)smem;
  volatile LAS unsigned* flags = (volatile LAS unsigned*)(L + L_FLAG);
  const int bid = blockIdx.x, tid = threadIdx.x, lane = tid & 63;
  const int wave = __builtin_amdgcn_readfirstlane(tid >> 6);
  const int xcd = bid & 7, li = bid >> 3;
  const int cu = xcd * 32 + li;
  const int h = cu >> 4, b = (cu >> 3) & 1, c = cu & 7, kh = c >> 2, dq = c & 3, bh = b * 16 + h;
  const int mb = li * 8 + xcd;
  const bool is_mod = mb < 42, is_ffn = li >= 6 && li < 13, is_red = li >= 13 && li < 27;
  const int fidx = xcd * 7 + li - 6;
  const int rb = (li - 13) & 1, rchunk = xcd * 7 + ((li - 13) >> 1);
  const rsrc_t rs = mkrsrc(p.ws, WS_END);
  STAMP(0);
  const unsigned e0 = __builtin_amdgcn_raw_buffer_load_b32(rs, WS_EPOCH, 0, AUX_SC1);
  if (tid < 8) flags[tid] = 0u;
  unsigned T;
  v4u bw[6];
  float kp[8];
#pragma unroll
  for (int j = 0; j < 8; ++j) kp[j] = 0.f;
  const int t0 = wave < 4 ? 16 + wave : wave - 4, t1 = wave < 4 ? 20 + wave : wave + 4, t2 = wave < 4 ? 24 + wave : 0;
  const int tr_row = 8 * (lane >> 4) + ((lane >> 2) & 3), tr_col = (lane & 3) * 8, g16 = lane >> 4;

  if (wave >= 4) {
    const int sw = wave - 4;
    const rsrc_t rk = mkrsrc(p.kc, 64 << 20), rv = mkrsrc(p.vc, 64 << 20);
    const int koff = (bh * SK + c * 512) * 512 + sw * 1024 + lane * 16;
    const int voff = (bh * SK + kh * 2048 + sw * 8 + (lane >> 3)) * 512 + dq * 128 + (lane & 7) * 16;
    v4u kb[32], vb[32];
#pragma unroll
    for (int i = 0; i < 32; ++i) {
      if (i == KDEPTH) bar();
      if (i >= KDEPTH) waitvm<KDEPTH - 1>();
      kb[i] = __builtin_amdgcn_raw_buffer_load_b128(rk, koff, i * 8192, AUX_NT);
      __builtin_amdgcn_sched_barrier(0);
    }
    STAMP(1);
    lds_wait(flags + F_Q, 1u);
    STAMP(2);
    const v4u qv = *(const LAS v4u*)(L + L_Q + (lane & 31) * 16);
    float s2[16];
#pragma unroll
    for (int m = 0; m < 16; ++m) {
      float a = 0.f, bb = 0.f;
#pragma unroll
      for (int j = 0; j < 4; ++j) { a = dot2(kb[2 * m][j], qv[j], a); bb = dot2(kb[2 * m + 1][j], qv[j], bb); }
      s2[m] = tr16(a, bb);
      if ((m & 3) == 3) {
        __builtin_amdgcn_sched_barrier(0);
        waitvm<VDEPTH - 1>();
        vb[m >> 2] = __builtin_amdgcn_raw_buffer_load_b128(rv, voff, (m >> 2) * 32768, AUX_NT);
        __builtin_amdgcn_sched_barrier(0);
      }
    }
    float s4[8], s8[4], s16[2];
#pragma unroll
    for (int u = 0; u < 8; ++u) s4[u] = trd<ROR8>(s2[2 * u], s2[2 * u + 1], lane & 8);
#pragma unroll
    for (int u = 0; u < 4; ++u) s8[u] = trd<XOR7>(s4[u], s4[u + 4], lane & 4);
#pragma unroll
    for (int u = 0; u < 2; ++u) s16[u] = trd<XOR2>(s8[u], s8[u + 2], lane & 2);
    const float sc = trd<XOR1>(s16[0], s16[1], lane & 1);
    T = __builtin_amdgcn_readfirstlane(e0) + 1u;
    {
      const unsigned pr = pack2(sc, dpp<XOR1>(sc));
      const unsigned pr2 = dppu<XOR2>(pr);
      if ((lane & 3) == 0) publish(rs, WS_SC + bh * 16384 + ((c * 8 + sw) * 16 + (lane >> 2)) * 16, pr, pr2, T);
      if (lane == 0) lds_inc(flags + F_SC);
    }
    STAMP(3);
    float acc[8];
#pragma unroll
    for (int j = 0; j < 8; ++j) acc[j] = 0.f;
    v4u pp[4];
#pragma unroll
    for (int i = 4; i <= 32; ++i) {
      if (i < 32) {
        __builtin_amdgcn_sched_barrier(0);
        waitvm<VDEPTH - 1>();
        vb[i] = __builtin_amdgcn_raw_buffer_load_b128(rv, voff, i * 32768, AUX_NT);
        __builtin_amdgcn_sched_barrier(0);
      }
      if (i == PSTART) {
        STAMP(4);
        lds_wait(flags + F_P, 1u);
        STAMP(5);
#pragma unroll
        for (int u = 0; u < 4; ++u) pp[u] = *(const LAS v4u*)(L + L_P + (sw * 8 + (lane >> 3)) * 64 + u * 16);
      }
      if (i == 30) {
        lds_wait(flags + F_WO, 1u);
#pragma unroll
        for (int s = 0; s < 2; ++s) {
          bw[s] = bfrag(L + L_WO + (32 * s + tr_row) * (D * 2) + t0 * 32 + tr_col, D * 2);
          bw[2 + s] = bfrag(L + L_WO + (32 * s + tr_row) * (D * 2) + t1 * 32 + tr_col, D * 2);
          bw[4 + s] = v4u{0u, 0u, 0u, 0u};
        }
      }
      const int m0 = pairs_done(i - 1), m1 = i < 32 ? pairs_done(i) : 16;
#pragma unroll
      for (int m = m0; m < m1; ++m) {
        const unsigned pw = pp[m >> 2][m & 3];
#pragma unroll
        for (int j = 0; j < 4; ++j) {
          const unsigned a = vb[2 * m][j], bb = vb[2 * m + 1][j];
          acc[2 * j] = dot2(__builtin_amdgcn_perm(bb, a, 0x05040100u), pw, acc[2 * j]);
          acc[2 * j + 1] = dot2(__builtin_amdgcn_perm(bb, a, 0x07060302u), pw, acc[2 * j + 1]);
        }
      }
    }
    STAMP(6);
    float a4[4], a2[2];
#pragma unroll
    for (int u = 0; u < 4; ++u) a4[u] = tr32(acc[u], acc[u + 4]);
#pragma unroll
    for (int u = 0; u < 2; ++u) a2[u] = tr16(a4[u], a4[u + 2]);
    const float o1 = trd<ROR8>(a2[0], a2[1], lane & 8);
    const int dim = (lane & 7) * 8 + ((lane >> 5) & 1) * 4 + ((lane >> 4) & 1) * 2 + ((lane >> 3) & 1);
    const float oh = rbf(o1);
    const unsigned hl = pack2(oh, o1 - oh);
    *(LAS u16*)(L + L_OA + sw * 144 + dim * 2) = (u16)hl;
    *(LAS u16*)(L + L_OA + (8 + sw) * 144 + dim * 2) = (u16)(hl >> 16);
  } else {
    const int a = wave;
    const rsrc_t rx = mkrsrc(p.x, 2 * D * 2);
    const int l56 = lane < 56 ? lane : 55;
    v4u xa, xb; unsigned cv0, cv1, xd0, xd1;
    const int rq = (cu & 15) >> 2, cbq = cu & 3;
    const int qk0 = rq < 2 ? rq * 128 : 64 + rq * 96, qnr = rq < 2 ? 128 : 96;
    const int qr = 2 * lane < qnr ? 2 * lane : qnr - 2;
    if (is_mod) {
      const rsrc_t rc = mkrsrc(p.c, 2 * D * 2);
      cv0 = __builtin_amdgcn_raw_buffer_load_b32(rc, (a * 112 + 2 * l56) * 2, 0, 0);
      cv1 = __builtin_amdgcn_raw_buffer_load_b32(rc, (D + a * 112 + 2 * l56) * 2, 0, 0);
      const GAS unsigned char* src = (const GAS unsigned char*)p.wmod + (size_t)(a * 112 + (lane >> 3)) * (6 * D * 2) + mb * 128 + (lane & 7) * 16;
#pragma unroll
      for (int i = 0; i < 14; ++i)
        __builtin_amdgcn_global_load_lds((const GAS unsigned*)(src + (size_t)i * 8 * (6 * D * 2)), (LAS unsigned*)(L + L_WM + (a * 14 + i) * 1024), 16, 0, POL_W);
    }
    if (a == 0) {
      xa = __builtin_amdgcn_raw_buffer_load_b128(rx, l56 * 16, 0, 0);
      xb = __builtin_amdgcn_raw_buffer_load_b128(rx, D * 2 + l56 * 16, 0, 0);
      xd0 = __builtin_amdgcn_raw_buffer_load_b32(rx, (qk0 + qr) * 2, 0, 0);
      xd1 = __builtin_amdgcn_raw_buffer_load_b32(rx, (D + qk0 + qr) * 2, 0, 0);
      const GAS unsigned char* src = (const GAS unsigned char*)p.wq + (size_t)(qk0 + (lane >> 3)) * (NH * DH * 2) + (h * 256 + cbq * 64) * 2 + (lane & 7) * 16;
#pragma unroll
      for (int i = 0; i < 16; ++i)
        if (i < 12 || rq < 2)
          __builtin_amdgcn_global_load_lds((const GAS unsigned*)(src + (size_t)i * 8 * (NH * DH * 2)), (LAS unsigned*)(L + L_WQ + i * 1024), 16, 0, POL_W);
    }
    bar();
    T = __builtin_amdgcn_readfirstlane(e0) + 1u;
    if (is_mod) {
      {
        const float c00 = lo16(cv0), c01 = hi16(cv0), c10 = lo16(cv1), c11 = hi16(cv1);
        const float s00 = c00 / (1.f + __expf(-c00)), s01 = c01 / (1.f + __expf(-c01));
        const float s10 = c10 / (1.f + __expf(-c10)), s11 = c11 / (1.f + __expf(-c11));
        *(LAS unsigned*)(L + L_H2 + (a * 112 + 2 * l56) * 2) = pack2(s00, s01);
        *(LAS unsigned*)(L + L_H2 + D * 2 + (a * 112 + 2 * l56) * 2) = pack2(s10, s11);
      }
      waitvm<0>();
      waitlds();
      if (lane == 0) lds_inc(flags + F_MOD);
      lds_wait(flags + F_MOD, 4u);
      v4f da = {0.f, 0.f, 0.f, 0.f}, db = {0.f, 0.f, 0.f, 0.f};
      LAS unsigned char* mq = L + L_WM + tr_row * 128 + a * 32 + tr_col;
#pragma unroll
      for (int s = 0; s < 14; s += 2) {
        const v4u a0 = *(const LAS v4u*)(L + L_H2 + (lane & 1) * (D * 2) + s * 64 + g16 * 16);
        const v4u a1 = *(const LAS v4u*)(L + L_H2 + (lane & 1) * (D * 2) + s * 64 + 64 + g16 * 16);
        da = mfma(a0, bfrag(mq + s * 4096, 128), da); db = mfma(a1, bfrag(mq + s * 4096 + 4096, 128), db);
      }
      const unsigned g = pack2(da[0] + db[0], da[1] + db[1]), gn = dppu<XOR1>(g);
      if (lane < 16 && (lane & 1) == 0) publish(rs, WS_MOD + (mb * 32 + a * 8 + (lane >> 1)) * 16, g, gn, T);
      STAMP(1);
    }
    if (a == 0) {
      waitvm<0>();
      v4u bq[16];
      {
        LAS unsigned char* qq = L + L_WQ + tr_row * 128 + tr_col;
#pragma unroll
        for (int s = 0; s < 4; ++s)
#pragma unroll
          for (int t = 0; t < 4; ++t) bq[s * 4 + t] = bfrag(qq + (s < 3 || rq < 2 ? s : 0) * 4096 + t * 32, 128);
      }
      waitlds();
      if (lane == 0) flags[F_WQ] = 1u;
      float sm0 = 0.f, sm1 = 0.f;
#pragma unroll
      for (int j = 0; j < 4; ++j) { sm0 += lo16(xa[j]) + hi16(xa[j]); sm1 += lo16(xb[j]) + hi16(xb[j]); }
      if (lane >= 56) { sm0 = 0.f; sm1 = 0.f; }
      const float mean0 = wave_sum(sm0) * (1.f / D), mean1 = wave_sum(sm1) * (1.f / D);
      float v0 = 0.f, v1 = 0.f;
#pragma unroll
      for (int j = 0; j < 4; ++j) {
        float d = lo16(xa[j]) - mean0; v0 = fmaf(d, d, v0); d = hi16(xa[j]) - mean0; v0 = fmaf(d, d, v0);
        d = lo16(xb[j]) - mean1; v1 = fmaf(d, d, v1); d = hi16(xb[j]) - mean1; v1 = fmaf(d, d, v1);
      }
      if (lane >= 56) { v0 = 0.f; v1 = 0.f; }
      const float rs0 = ln_rsd(wave_sum(v0) * (1.f / D)), rs1 = ln_rsd(wave_sum(v1) * (1.f / D));
      const float mb0 = rbf(mean0), mb1 = rbf(mean1);
      const float n00 = rbf(rbf(lo16(xd0) - mb0) * rs0), n01 = rbf(rbf(hi16(xd0) - mb0) * rs0);
      const float n10 = rbf(rbf(lo16(xd1) - mb1) * rs1), n11 = rbf(rbf(hi16(xd1) - mb1) * rs1);
      {
        int off[2] = {WS_MOD + ((qk0 + qr) >> 1) * 16, WS_MOD + (224 + ((qk0 + qr) >> 1)) * 16}; v4u g[2];
        gather<2, PSLP>(rs, off, g, T);
        STAMP(2);
        const float h00 = rbf(n00 * rbf(1.f + lo16(g[1].x))) + lo16(g[0].x), h10 = rbf(n10 * rbf(1.f + hi16(g[1].x))) + hi16(g[0].x);
        const float h01 = rbf(n01 * rbf(1.f + lo16(g[1].z))) + lo16(g[0].z), h11 = rbf(n11 * rbf(1.f + hi16(g[1].z))) + hi16(g[0].z);
        const bool live = 2 * lane < qnr;
        *(LAS unsigned*)(L + L_H1 + lane * 4) = live ? pack2(h00, h01) : 0u;
        *(LAS unsigned*)(L + L_H1 + 256 + lane * 4) = live ? pack2(h10, h11) : 0u;
      }
      {
        v4f d[4];
#pragma unroll
        for (int t = 0; t < 4; ++t) d[t] = v4f{0.f, 0.f, 0.f, 0.f};
#pragma unroll
        for (int s = 0; s < 4; ++s) {
          const v4u aa = *(const LAS v4u*)(L + L_H1 + (lane & 1) * 256 + s * 64 + g16 * 16);
#pragma unroll
          for (int t = 0; t < 4; ++t) d[t] = mfma(aa, bq[s * 4 + t], d[t]);
        }
        const float q0 = g16 == 0 ? d[0][0] : (g16 == 1 ? d[1][0] : (g16 == 2 ? d[2][0] : d[3][0]));
        const float q1 = g16 == 0 ? d[0][1] : (g16 == 1 ? d[1][1] : (g16 == 2 ? d[2][1] : d[3][1]));
        const bool odd = lane & 1;
        const float rcv = dpp<XOR1>(odd ? q0 : q1);
        publish(rs, WS_Q + ((((lane & 1) * 16 + h) * 4 + rq) * 128 + cbq * 32 + (lane >> 1)) * 16,
                __float_as_uint(odd ? rcv : q0), __float_as_uint(odd ? q1 : rcv), T);
      }
      STAMP(3);
      {
        int off[8]; v4u g[8];
#pragma unroll
        for (int k = 0; k < 8; ++k) off[k] = WS_Q + bh * 8192 + (k * 64 + lane) * 16;
        gather<8, PSLP>(rs, off, g, T);
#pragma unroll
        for (int u = 0; u < 2; ++u) {
          const float qa = (__uint_as_float(g[u].x) + __uint_as_float(g[2 + u].x)) + (__uint_as_float(g[4 + u].x) + __uint_as_float(g[6 + u].x));
          const float qb = (__uint_as_float(g[u].z) + __uint_as_float(g[2 + u].z)) + (__uint_as_float(g[4 + u].z) + __uint_as_float(g[6 + u].z));
          *(LAS unsigned*)(L + L_Q + (u * 64 + lane) * 4) = pack2(qa, qb);
        }
        waitlds();
        if (lane == 0) flags[F_Q] = 1u;
      }
      STAMP(4);
      lds_wait(flags + F_SC, 8u);
      {
        int off[16]; v4u g[16];
#pragma unroll
        for (int k = 0; k < 16; ++k) off[k] = WS_SC + bh * 16384 + (((k ^ (kh * 8)) * 64) + lane) * 16;
        gather<16, PSLP>(rs, off, g, T);
        STAMP(5);
        float m = -INFINITY;
#pragma unroll
        for (int k = 0; k < 16; ++k) m = fmaxf(fmaxf(fmaxf(lo16(g[k].x), hi16(g[k].x)), fmaxf(lo16(g[k].z), hi16(g[k].z))), m);
        m = wave_max(m);
        const float cs = 0.0625f * 1.4426950408889634f;
        const float mc = m * cs;
        float e[32]; float l = 0.f;
#pragma unroll
        for (int k = 0; k < 16; ++k) {
          const float e0_ = __builtin_amdgcn_exp2f(fmaf(lo16(g[k].x), cs, -mc)), e1_ = __builtin_amdgcn_exp2f(fmaf(hi16(g[k].x), cs, -mc));
          const float e2_ = __builtin_amdgcn_exp2f(fmaf(lo16(g[k].z), cs, -mc)), e3_ = __builtin_amdgcn_exp2f(fmaf(hi16(g[k].z), cs, -mc));
          l += (e0_ + e1_) + (e2_ + e3_);
          if (k < 8) { e[4 * k] = e0_; e[4 * k + 1] = e1_; e[4 * k + 2] = e2_; e[4 * k + 3] = e3_; }
        }
        l = wave_sum(l);
        const float rl = rcp_nr(l);
        const int t = lane & 15;
        const int half = t >> 3, j0 = (t >> 2) & 1, j1 = (t >> 1) & 1, j4 = t & 1;
#pragma unroll
        for (int k = 0; k < 8; ++k) {
          const int cq = k >> 1;
          const int wp = (k & 1) * 4 + (lane >> 4);
          const int wc = (j1 << 2) | (j0 << 1) | (wp >> 2), gg = ((wp & 3) << 1) | half, mm = (cq << 2) | (j4 << 1);
          *(LAS v2u*)(L + L_P + ((wc * 8 + gg) * 16 + mm) * 4) = v2u{pack2(e[4 * k] * rl, e[4 * k + 1] * rl), pack2(e[4 * k + 2] * rl, e[4 * k + 3] * rl)};
        }
        waitlds();
        if (lane == 0) flags[F_P] = 1u;
      }
      STAMP(6);
    } else if (a == 1) {
      const GAS unsigned char* src = (const GAS unsigned char*)p.wo + (size_t)(h * 256 + dq * 64) * (D * 2) + lane * 16;
#pragma unroll
      for (int i = 0; i < 56; ++i) {
        if (i >= WDEPTH) waitvm<WDEPTH - 1>();
        __builtin_amdgcn_global_load_lds((const GAS unsigned*)(src + i * 1024), (LAS unsigned*)(L + L_WO + i * 1024), 16, 0, POL_W);
      }
      waitvm<0>();
      if (lane == 0) flags[F_WO] = 1u;
      STAMP(2);
    } else if (a == 2) {
      if (is_ffn) {
        const GAS unsigned char* gsrc = (const GAS unsigned char*)((lane & 4) ? p.wu : p.wg) + (size_t)(lane >> 3) * (F * 2) + fidx * 64 + (lane & 3) * 16;
#pragma unroll
        for (int i = 0; i < 56; ++i) {
          if (i >= WDEPTH) waitvm<WDEPTH - 1>();
          __builtin_amdgcn_global_load_lds((const GAS unsigned*)(gsrc + (size_t)i * 8 * (F * 2)), (LAS unsigned*)(L + L_GU + i * 1024), 16, 0, POL_W);
        }
        waitvm<0>();
        STAMP(2);
      }
    } else {
      if (is_ffn) {
        lds_wait(flags + F_WQ, 1u);
        const GAS unsigned char* dsrc = (const GAS unsigned char*)p.wd + (size_t)fidx * 32 * (D * 2) + lane * 16;
#pragma unroll
        for (int i = 0; i < 28; ++i) {
          if (i >= WDEPTH) waitvm<WDEPTH - 1>();
          __builtin_amdgcn_global_load_lds((const GAS unsigned*)(dsrc + i * 1024), (LAS unsigned*)(L + L_DN + i * 1024), 16, 0, AUX_NT);
        }
        waitvm<0>();
        STAMP(2);
      }
      if (is_red) {
        const unsigned xr = __builtin_amdgcn_raw_buffer_load_b32(rx, (rb * D + rchunk * 8 + (lane & 3) * 2) * 2, 0, 0);
        int off[2] = {WS_MOD + (D + rchunk * 4 + (lane & 3)) * 16, WS_MOD + (5 * D / 2 + rchunk * 4 + (lane & 3)) * 16}; v4u g[2];
        gather<2, 32>(rs, off, g, T);
        kp[0] = rb ? hi16(g[0].x) : lo16(g[0].x); kp[1] = rb ? hi16(g[0].z) : lo16(g[0].z);
        kp[2] = rb ? hi16(g[1].x) : lo16(g[1].x); kp[3] = rb ? hi16(g[1].z) : lo16(g[1].z);
        kp[4] = lo16(xr); kp[5] = hi16(xr);
        STAMP(2);
      }
    }
    if (is_ffn) {
      const int col2 = ((a & 1) * 56 + l56) * 2;
      int off[4] = {WS_MOD + (2 * D + col2) * 16, WS_MOD + (2 * D + col2 + 1) * 16, WS_MOD + (3 * D / 2 + col2) * 16, WS_MOD + (3 * D / 2 + col2 + 1) * 16};
      v4u g[4];
      gather<4, 32>(rs, off, g, T);
      const bool hb = a >> 1;
      kp[0] = rbf(1.f + (hb ? hi16(g[0].x) : lo16(g[0].x))); kp[1] = rbf(1.f + (hb ? hi16(g[0].z) : lo16(g[0].z)));
      kp[2] = rbf(1.f + (hb ? hi16(g[1].x) : lo16(g[1].x))); kp[3] = rbf(1.f + (hb ? hi16(g[1].z) : lo16(g[1].z)));
      kp[4] = hb ? hi16(g[2].x) : lo16(g[2].x); kp[5] = hb ? hi16(g[2].z) : lo16(g[2].z);
      kp[6] = hb ? hi16(g[3].x) : lo16(g[3].x); kp[7] = hb ? hi16(g[3].z) : lo16(g[3].z);
    }
    lds_wait(flags + F_WO, 1u);
#pragma unroll
    for (int s = 0; s < 2; ++s) {
      bw[s] = bfrag(L + L_WO + (32 * s + tr_row) * (D * 2) + t0 * 32 + tr_col, D * 2);
      bw[2 + s] = bfrag(L + L_WO + (32 * s + tr_row) * (D * 2) + t1 * 32 + tr_col, D * 2);
      bw[4 + s] = bfrag(L + L_WO + (32 * s + tr_row) * (D * 2) + t2 * 32 + tr_col, D * 2);
    }
    STAMP(7);
  }

  bar();
  const int n16 = lane & 15;
  const int tsel = g16 == 0 ? t0 : (g16 == 2 ? t1 : t2);
  {
    const v4u a0 = *(const LAS v4u*)(L + L_OA + n16 * 144 + g16 * 16), a1 = *(const LAS v4u*)(L + L_OA + n16 * 144 + 64 + g16 * 16);
    const v4f z = {0.f, 0.f, 0.f, 0.f};
    const v4f d0 = mfma(a1, bw[1], mfma(a0, bw[0], z)), d1 = mfma(a1, bw[3], mfma(a0, bw[2], z)), d2 = mfma(a1, bw[5], mfma(a0, bw[4], z));
    const float u1 = tr32((d0[0] + d0[1]) + (d0[2] + d0[3]), (d1[0] + d1[1]) + (d1[2] + d1[3]));
    const float u2 = sum32((d2[0] + d2[1]) + (d2[2] + d2[3]));
    const float v = tr16(u1, u2);
    const float vn = dpp<XOR1>(v);
    const int n = tsel * 16 + n16;
    if ((lane & 1) == 0 && ((g16 & 1) == 0 || (g16 == 1 && wave < 4)))
      publish(rs, WS_P1 + (((b * 56 + (n >> 3)) * 128 + h * 8 + c) * 4 + ((n >> 1) & 3)) * 16, __float_as_uint(v), __float_as_uint(vn), T);
    STAMP(8);
  }
  if (is_red) {
    if (wave == 3) {
      float x20, x21;
      {
        int off[8]; v4u g[8];
#pragma unroll
        for (int k = 0; k < 8; ++k) off[k] = WS_P1 + (rb * 56 + rchunk) * 8192 + (k * 64 + lane) * 16;
        gather<8, 1>(rs, off, g, T);
        STAMP(9);
        float s0 = 0.f, s1 = 0.f;
#pragma unroll
        for (int k = 0; k < 8; k += 2) { s0 += __uint_as_float(g[k].x) + __uint_as_float(g[k + 1].x); s1 += __uint_as_float(g[k].z) + __uint_as_float(g[k + 1].z); }
        s0 += dpp<ROR4>(s0); s1 += dpp<ROR4>(s1); s0 += dpp<ROR8>(s0); s1 += dpp<ROR8>(s1);
        s0 = sum16(s0); s1 = sum16(s1); s0 = sum32(s0); s1 = sum32(s1);
        x20 = rbf(kp[4] + rbf(kp[0] * rbf(s0))); x21 = rbf(kp[5] + rbf(kp[1] * rbf(s1)));
        const unsigned xp = pack2(x20, x21), xq = dppu<XOR1>(xp);
        float sm = x20 + x21; sm += dpp<XOR1>(sm); sm += dpp<XOR2>(sm);
        const float d0 = x20 - sm * 0.125f, d1 = x21 - sm * 0.125f;
        float m2 = fmaf(d0, d0, d1 * d1); m2 += dpp<XOR1>(m2); m2 += dpp<XOR2>(m2);
        const bool stl = lane == 1;
        if (lane < 3) publish(rs, stl ? WS_ST + (rb * 56 + rchunk) * 16 : WS_X2 + (rb * 112 + rchunk * 2 + (lane >> 1)) * 16,
                              stl ? __float_as_uint(sm) : xp, stl ? __float_as_uint(m2) : xq, T);
        STAMP(10);
      }
      {
        int off[4]; v4u g[4];
#pragma unroll
        for (int k = 0; k < 4; ++k) { int pc = k * 64 + lane; pc = pc < 224 ? pc : pc - 32; off[k] = WS_P2 + (rb * 56 + rchunk) * (NF * 64) + pc * 16; }
        gather<4, 1>(rs, off, g, T);
        float s0 = (__uint_as_float(g[0].x) + __uint_as_float(g[1].x)) + (__uint_as_float(g[2].x) + (lane < 32 ? __uint_as_float(g[3].x) : 0.f));
        float s1 = (__uint_as_float(g[0].z) + __uint_as_float(g[1].z)) + (__uint_as_float(g[2].z) + (lane < 32 ? __uint_as_float(g[3].z) : 0.f));
        s0 += dpp<ROR4>(s0); s1 += dpp<ROR4>(s1); s0 += dpp<ROR8>(s0); s1 += dpp<ROR8>(s1);
        s0 = sum16(s0); s1 = sum16(s1); s0 = sum32(s0); s1 = sum32(s1);
        const unsigned ov = pack2(x20 + rbf(kp[2] * rbf(s0)), x21 + rbf(kp[3] * rbf(s1)));
        if (lane < 4) ((GAS unsigned*)p.out)[(rb * D + rchunk * 8) / 2 + lane] = ov;
        if (bid == 13 * 8 && lane == 0) __builtin_amdgcn_raw_buffer_store_b32(T, rs, WS_EPOCH, 0, AUX_SC1);
        STAMP(11);
      }
    }
  } else if (is_ffn) {
    v4u bd[3];
    bd[0] = bfrag(L + L_DN + tr_row * (D * 2) + t0 * 32 + tr_col, D * 2);
    bd[1] = bfrag(L + L_DN + tr_row * (D * 2) + t1 * 32 + tr_col, D * 2);
    bd[2] = bfrag(L + L_DN + tr_row * (D * 2) + t2 * 32 + tr_col, D * 2);
    if (wave < 4) {
      v4u bg[14];
      {
        LAS unsigned char* gq = L + L_GU + tr_row * 128 + (lane & 2) * 32 + wave * 16 + (lane & 1) * 8;
#pragma unroll
        for (int s = 0; s < 14; ++s) bg[s] = bfrag(gq + s * 32 * 128, 128);
      }
      const int l56 = lane < 56 ? lane : 55, xbt = wave >> 1;
      {
        int off[2] = {WS_X2 + (wave * 56 + l56) * 16, WS_ST + (xbt * 56 + l56) * 16}; v4u g[2];
        gather<2, 1>(rs, off, g, T);
        STAMP(9);
        const float sm = lane < 56 ? __uint_as_float(g[1].x) : 0.f;
        const float mean = wave_sum(sm) * (1.f / D);
        const float dm = sm * 0.125f - mean;
        const float m2 = wave_sum(lane < 56 ? fmaf(8.f * dm, dm, __uint_as_float(g[1].z)) : 0.f);
        const float rsd = ln_rsd(m2 * (1.f / D)), mbf = rbf(mean);
        const float h0 = rbf(rbf(rbf(lo16(g[0].x) - mbf) * rsd) * kp[0]) + kp[4], h1 = rbf(rbf(rbf(hi16(g[0].x) - mbf) * rsd) * kp[1]) + kp[5];
        const float h2 = rbf(rbf(rbf(lo16(g[0].z) - mbf) * rsd) * kp[2]) + kp[6], h3 = rbf(rbf(rbf(hi16(g[0].z) - mbf) * rsd) * kp[3]) + kp[7];
        *(LAS v2u*)(L + L_H2 + (wave * 56 + l56) * 8) = v2u{pack2(h0, h1), pack2(h2, h3)};
      }
      bar();
      v4f da = {0.f, 0.f, 0.f, 0.f}, db = {0.f, 0.f, 0.f, 0.f};
#pragma unroll
      for (int s = 0; s < 14; s += 2) {
        const v4u a0 = *(const LAS v4u*)(L + L_H2 + (lane & 1) * (D * 2) + s * 64 + g16 * 16);
        const v4u a1 = *(const LAS v4u*)(L + L_H2 + (lane & 1) * (D * 2) + s * 64 + 64 + g16 * 16);
        da = mfma(a0, bg[s], da); db = mfma(a1, bg[s + 1], db);
      }
      const float g0 = rbf(da[0] + db[0]), g1 = rbf(da[1] + db[1]);
      const float u0 = dpp<ROR8>(g0), u1 = dpp<ROR8>(g1);
      const float k1 = -2.f * 0.7978845608028654f * 1.4426950408889634f;
      const float e0_ = __builtin_amdgcn_exp2f(k1 * fmaf(0.044715f * g0 * g0, g0, g0)), e1_ = __builtin_amdgcn_exp2f(k1 * fmaf(0.044715f * g1 * g1, g1, g1));
      const float ge0 = rbf(g0 * __builtin_amdgcn_rcpf(1.f + e0_)), ge1 = rbf(g1 * __builtin_amdgcn_rcpf(1.f + e1_));
      const unsigned av = pack2(ge0 * u0, ge1 * u1);
      if (lane < 8) {
        *(LAS u16*)(L + L_ACT + (wave * 8 + lane) * 2) = (u16)av;
        *(LAS u16*)(L + L_ACT + 64 + (wave * 8 + lane) * 2) = (u16)(av >> 16);
      }
      STAMP(10);
    } else {
      bar();
    }
    bar();
    {
      const v4u aa = *(const LAS v4u*)(L + L_ACT + (lane & 1) * 64 + g16 * 16);
      const v4f z = {0.f, 0.f, 0.f, 0.f};
      const v4f d0 = mfma(aa, bd[0], z), d1 = mfma(aa, bd[1], z), d2 = mfma(aa, bd[2], z);
      const float v0 = g16 == 0 ? d0[0] : (g16 == 2 ? d1[0] : d2[0]), v1 = g16 == 0 ? d0[1] : (g16 == 2 ? d1[1] : d2[1]);
      const bool odd = lane & 1;
      const float rcv = dpp<XOR1>(odd ? v0 : v1);
      const int n = tsel * 16 + n16;
      if ((g16 & 1) == 0 || (g16 == 1 && wave < 4))
        publish(rs, WS_P2 + ((((lane & 1) * 56 + (n >> 3)) * NF + fidx) * 4 + ((n >> 1) & 3)) * 16,
                __float_as_uint(odd ? rcv : v0), __float_as_uint(odd ? v1 : rcv), T);
      STAMP(11);
    }
  }
  STAMP_FLUSH();
}
}

extern "C" void kernel_launch(void* const* d_in, const int* in_sizes, int n_in, void* d_out, int out_size, void* d_ws,
                              size_t ws_size, hipStream_t stream) {
  Params p;
  p.x = (const u16*)d_in[0]; p.kc = (const u16*)d_in[1]; p.vc = (const u16*)d_in[2];
  p.wq = (const u16*)d_in[3]; p.wo = (const u16*)d_in[4];
  p.wg = (const u16*)d_in[6]; p.wu = (const u16*)d_in[7]; p.wd = (const u16*)d_in[8];
  p.c = (const u16*)d_in[9]; p.wmod = (const u16*)d_in[10];
  p.out = (u16*)d_out; p.ws = (unsigned char*)d_ws;
  (void)hipFuncSetAttribute((const void*)fused_block, hipFuncAttributeMaxDynamicSharedMemorySize, L_END);
  hipLaunchKernelGGL(fused_block, dim3(256), dim3(NTHR), L_END, stream, p);
}
```

```cpp
#include <hip/hip_runtime.h>
#include <stdint.h>

typedef unsigned short u16;
typedef unsigned v4u __attribute__((ext_vector_type(4)));
typedef unsigned v2u __attribute__((ext_vector_type(2)));
typedef float v4f __attribute__((ext_vector_type(4)));
typedef float v2f __attribute__((ext_vector_type(2)));
typedef short v8s __attribute__((ext_vector_type(8)));
typedef short v4s __attribute__((ext_vector_type(4)));
typedef __bf16 bf16x2 __attribute__((ext_vector_type(2)));
#define GAS __attribute__((address_space(1)))
#define LAS __attribute__((address_space(3)))

#define KDEPTH 3
#define VDEPTH 4
#define WDEPTH 6
#define PSTART 24
#define FSTART 28
#define FDEPTH 16
#define HOLDK 14
#define PSLP 8
#define POL_K 2
#define POL_V 2
#define POL_W 0
#define DBG_STAMP 0

namespace {
constexpr int D = 448, NH = 16, SK = 4096, DH = 256, F = 1792;
constexpr int NTHR = 768;
constexpr int NF = 56;
constexpr int AUX_NT = 2, AUX_SC1 = 16;
constexpr unsigned SPIN_CAP = 1u << 16;

constexpr int WS_EPOCH = 0;
constexpr int WS_MOD = 256;
constexpr int WS_X2 = 22528;
constexpr int WS_ST = 26624;
constexpr int WS_Q = 32768;
constexpr int WS_SC = WS_Q + 32 * 8192;
constexpr int WS_P1 = WS_SC + 32 * 16384;
constexpr int WS_P2 = WS_P1 + 2 * 56 * 8192;
constexpr int WS_END = WS_P2 + 2 * 56 * NF * 64;
constexpr int WS_STAMP = 4 << 20;
static_assert(WS_END <= WS_STAMP, "ws map");

constexpr int L_WO = 0;
constexpr int L_GU = 57344;
constexpr int L_DN = L_GU + 57344;
constexpr int L_Q = L_DN + 28672;
constexpr int L_H1 = L_Q + 512;
constexpr int L_P = L_H1 + 512;
constexpr int L_OA = L_P + 4096;
constexpr int L_H2 = L_OA + 2304;
constexpr int L_WM = L_GU;
constexpr int L_WQ = L_DN;
constexpr int L_ACT = L_H2 + 1792;
constexpr int L_FLAG = L_ACT + 128;
constexpr int L_END = L_FLAG + 64;
static_assert(L_END <= 160 * 1024 && L_END > 80 * 1024, "lds");
enum { F_Q = 0, F_P = 1, F_WO = 2, F_SC = 3, F_MOD = 4, F_KI = 5, F_VI = 6, F_FW = 7 };

struct Params {
  const u16 *x, *kc, *vc, *wq, *wo, *wg, *wu, *wd, *c, *wmod;
  u16* out;
  unsigned char* ws;
};

__device__ __forceinline__ float lo16(unsigned w) { return __uint_as_float(w << 16); }
__device__ __forceinline__ float hi16(unsigned w) { return __uint_as_float(w & 0xffff0000u); }
__device__ __forceinline__ unsigned pack2(float a, float b) {
  bf16x2 v = {(__bf16)a, (__bf16)b};
  return __builtin_bit_cast(unsigned, v);
}
__device__ __forceinline__ float rbf(float f) { return lo16(pack2(f, f)); }
__device__ __forceinline__ float dot2(unsigned a, unsigned b, float c) {
  return __builtin_amdgcn_fdot2_f32_bf16(__builtin_bit_cast(bf16x2, a), __builtin_bit_cast(bf16x2, b), c, false);
}
__device__ __forceinline__ float rcp_nr(float x) { const float r = __builtin_amdgcn_rcpf(x); return fmaf(fmaf(-x, r, 1.f), r, r); }
template <int N> __device__ __forceinline__ void waitvm() {
  asm volatile("" ::: "memory");
  __builtin_amdgcn_s_waitcnt((N & 15) | ((N >> 4) << 14) | 0x0F70);
  asm volatile("" ::: "memory");
}
__device__ __forceinline__ void waitlds() { asm volatile("s_waitcnt lgkmcnt(0)" ::: "memory"); }
__device__ __forceinline__ void bar() {
  __builtin_amdgcn_sched_barrier(0);
  asm volatile("s_waitcnt lgkmcnt(0)" ::: "memory");
  __builtin_amdgcn_s_barrier();
  asm volatile("" ::: "memory");
  __builtin_amdgcn_sched_barrier(0);
}
template <int CTRL> __device__ __forceinline__ float dpp(float x) {
  return __builtin_bit_cast(float, __builtin_amdgcn_mov_dpp(__builtin_bit_cast(int, x), CTRL, 0xf, 0xf, true));
}
template <int CTRL> __device__ __forceinline__ unsigned dppu(unsigned x) {
  return (unsigned)__builtin_amdgcn_mov_dpp((int)x, CTRL, 0xf, 0xf, true);
}
constexpr int XOR1 = 0xB1, XOR2 = 0x4E, XOR7 = 0x141, ROR8 = 0x128, ROR4 = 0x124;
__device__ __forceinline__ float tr32(float a, float b) {
  auto r = __builtin_amdgcn_permlane32_swap(__float_as_uint(a), __float_as_uint(b), false, false);
  return __uint_as_float(r[0]) + __uint_as_float(r[1]);
}
__device__ __forceinline__ float tr16(float a, float b) {
  auto r = __builtin_amdgcn_permlane16_swap(__float_as_uint(a), __float_as_uint(b), false, false);
  return __uint_as_float(r[0]) + __uint_as_float(r[1]);
}
template <int CTRL> __device__ __forceinline__ float trd(float a, float b, bool bit) {
  const float t1 = a + dpp<CTRL>(a), t2 = b + dpp<CTRL>(b);
  return bit ? t2 : t1;
}
__device__ __forceinline__ float sum32(float a) { return tr32(a, a); }
__device__ __forceinline__ float sum16(float a) { return tr16(a, a); }
__device__ __forceinline__ float wave_sum(float v) {
  v += dpp<XOR1>(v); v += dpp<XOR2>(v); v += dpp<XOR7>(v); v += dpp<ROR8>(v);
  v = sum16(v); v = sum32(v);
  return v;
}
__device__ __forceinline__ float wave_max(float v) {
  v = fmaxf(v, dpp<XOR1>(v)); v = fmaxf(v, dpp<XOR2>(v)); v = fmaxf(v, dpp<XOR7>(v)); v = fmaxf(v, dpp<ROR8>(v));
  auto r = __builtin_amdgcn_permlane16_swap(__float_as_uint(v), __float_as_uint(v), false, false);
  v = fmaxf(__uint_as_float(r[0]), __uint_as_float(r[1]));
  auto t = __builtin_amdgcn_permlane32_swap(__float_as_uint(v), __float_as_uint(v), false, false);
  return fmaxf(__uint_as_float(t[0]), __uint_as_float(t[1]));
}
__device__ __forceinline__ v4f mfma(v4u a, v4u b, v4f c) {
  return __builtin_amdgcn_mfma_f32_16x16x32_bf16(__builtin_bit_cast(v8s, a), __builtin_bit_cast(v8s, b), c, 0, 0, 0);
}
__device__ __forceinline__ v4u bfrag(LAS unsigned char* q, int stride) {
  const v2u a = __builtin_bit_cast(v2u, __builtin_amdgcn_ds_read_tr16_b64_v4i16((LAS v4s*)q));
  const v2u b = __builtin_bit_cast(v2u, __builtin_amdgcn_ds_read_tr16_b64_v4i16((LAS v4s*)(q + 4 * stride)));
  return v4u{a.x, a.y, b.x, b.y};
}

typedef __amdgpu_buffer_rsrc_t rsrc_t;
__device__ __forceinline__ rsrc_t mkrsrc(const void* p, int bytes) { return __builtin_amdgcn_make_buffer_rsrc((void*)p, 0, bytes, 0x00020000); }
__device__ __forceinline__ void publish(rsrc_t rs, int off, unsigned a, unsigned b, unsigned T) {
  v4u v = {a, T, b, T};
  __builtin_amdgcn_raw_buffer_store_b128(v, rs, off, 0, AUX_SC1);
}
template <int N, int SLP> __device__ __forceinline__ void gather(rsrc_t rs, const int (&off)[N], v4u (&v)[N], unsigned T) {
#pragma unroll
  for (int k = 0; k < N; ++k) v[k] = __builtin_amdgcn_raw_buffer_load_b128(rs, off[k], 0, AUX_SC1);
  for (unsigned spins = 0; spins < SPIN_CAP; ++spins) {
    bool ok = true;
#pragma unroll
    for (int k = 0; k < N; ++k) ok &= (v[k].y == T) & (v[k].w == T);
    if (__all(ok)) break;
    __builtin_amdgcn_s_sleep(SLP);
    asm volatile("" ::: "memory");
#pragma unroll
    for (int k = 0; k < N; ++k)
      if ((v[k].y != T) | (v[k].w != T)) v[k] = __builtin_amdgcn_raw_buffer_load_b128(rs, off[k], 0, AUX_SC1);
  }
}
__device__ __forceinline__ void lds_wait(volatile LAS unsigned* f, unsigned want) {
  for (unsigned spins = 0; spins < (1u << 22); ++spins) {
    if (__builtin_amdgcn_readfirstlane(*f) >= want) break;
    __builtin_amdgcn_s_sleep(1);
  }
  asm volatile("" ::: "memory");
}
__device__ __forceinline__ void lds_inc(volatile LAS unsigned* f) {
  __hip_atomic_fetch_add((LAS unsigned*)f, 1u, __ATOMIC_RELAXED, __HIP_MEMORY_SCOPE_WORKGROUP);
}
constexpr int pairs_done(int i) {
  if (i < PSTART) return 0;
  int a = (i - VDEPTH + 1) / 2, b = (i - PSTART + 1) * 3;
  return a < b ? a : b;
}
__device__ __forceinline__ float ln_rsd(float var) { return rcp_nr(rbf(__builtin_amdgcn_sqrtf(rbf(rbf(var) + 1e-5f)))); }

#define STAMP(i)
#define STAMP_FLUSH()

__global__ void __launch_bounds__(NTHR, 3) fused_block(Params p) {
  extern __shared__ __attribute__((aligned(16))) unsigned char smem[];
  LAS unsigned char* L = (LAS unsigned char*)smem;
  volatile LAS unsigned* flags = (volatile LAS unsigned*)(L + L_FLAG);
  const int bid = blockIdx.x, tid = threadIdx.x, lane = tid & 63;
  const int wave = __builtin_amdgcn_readfirstlane(tid >> 6);
  const int xcd = bid & 7, li = bid >> 3;
  const int cu = xcd * 32 + li;
  const int h = cu >> 4, b = (cu >> 3) & 1, c = cu & 7, kh = c >> 2, dq = c & 3, bh = b * 16 + h;
  const int mb = li * 8 + xcd;
  const bool is_mod = mb < 42, is_ffn = li >= 6 && li < 13, is_red = li >= 13 && li < 27;
  const int fidx = xcd * 7 + li - 6;
  const int rb = (li - 13) & 1, rchunk = xcd * 7 + ((li - 13) >> 1);
  const rsrc_t rs = mkrsrc(p.ws, WS_END);
  STAMP(0);
  const unsigned e0 = __builtin_amdgcn_raw_buffer_load_b32(rs, WS_EPOCH, 0, AUX_SC1);
  if (tid < 8) flags[tid] = 0u;
  unsigned T;
  v4u bw[6];
  float kp[8];
#pragma unroll
  for (int j = 0; j < 8; ++j) kp[j] = 0.f;
  const int t0 = wave < 4 ? 16 + wave : wave - 4, t1 = wave < 4 ? 20 + wave : wave + 4, t2 = wave < 4 ? 24 + wave : 0;
  const int tr_row = 8 * (lane >> 4) + ((lane >> 2) & 3), tr_col = (lane & 3) * 8, g16 = lane >> 4;

  if (wave >= 4) {
    const int sw = wave - 4;
    const rsrc_t rk = mkrsrc(p.kc, 64 << 20), rv = mkrsrc(p.vc, 64 << 20);
    const int koff = (bh * SK + c * 512) * 512 + sw * 1024 + lane * 16;
    const int voff = (bh * SK + kh * 2048 + sw * 8 + (lane >> 3)) * 512 + dq * 128 + (lane & 7) * 16;
    v4u kb[32], vb[32];
    bar();
    if (bid < HOLDK) lds_wait(flags + F_MOD, 4u);
#pragma unroll
    for (int i = 0; i < 32; ++i) {
      if (i >= KDEPTH) waitvm<KDEPTH - 1>();
      kb[i] = __builtin_amdgcn_raw_buffer_load_b128(rk, koff, i * 8192, POL_K);
      __builtin_amdgcn_sched_barrier(0);
    }
    if (sw == 0 && lane == 0) flags[F_KI] = 1u;
    STAMP(1);
    lds_wait(flags + F_Q, 1u);
    STAMP(2);
    const v4u qv = *(const LAS v4u*)(L + L_Q + (lane & 31) * 16);
    float s2[16];
#pragma unroll
    for (int m = 0; m < 16; ++m) {
      float a = 0.f, bb = 0.f;
#pragma unroll
      for (int j = 0; j < 4; ++j) { a = dot2(kb[2 * m][j], qv[j], a); bb = dot2(kb[2 * m + 1][j], qv[j], bb); }
      s2[m] = tr16(a, bb);
      if ((m & 3) == 3) {
        __builtin_amdgcn_sched_barrier(0);
        waitvm<VDEPTH - 1>();
        vb[m >> 2] = __builtin_amdgcn_raw_buffer_load_b128(rv, voff, (m >> 2) * 32768, POL_V);
        __builtin_amdgcn_sched_barrier(0);
      }
    }
    float s4[8], s8[4], s16[2];
#pragma unroll
    for (int u = 0; u < 8; ++u) s4[u] = trd<ROR8>(s2[2 * u], s2[2 * u + 1], lane & 8);
#pragma unroll
    for (int u = 0; u < 4; ++u) s8[u] = trd<XOR7>(s4[u], s4[u + 4], lane & 4);
#pragma unroll
    for (int u = 0; u < 2; ++u) s16[u] = trd<XOR2>(s8[u], s8[u + 2], lane & 2);
    const float sc = trd<XOR1>(s16[0], s16[1], lane & 1);
    T = __builtin_amdgcn_readfirstlane(e0) + 1u;
    {
      const unsigned pr = pack2(sc, dpp<XOR1>(sc));
      const unsigned pr2 = dppu<XOR2>(pr);
      if ((lane & 3) == 0) publish(rs, WS_SC + bh * 16384 + ((c * 8 + sw) * 16 + (lane >> 2)) * 16, pr, pr2, T);
      if (lane == 0) lds_inc(flags + F_SC);
    }
    STAMP(3);
    float acc[8];
#pragma unroll
    for (int j = 0; j < 8; ++j) acc[j] = 0.f;
    v4u pp[4];
#pragma unroll
    for (int i = 4; i <= 32; ++i) {
      if (i < 32) {
        __builtin_amdgcn_sched_barrier(0);
        waitvm<VDEPTH - 1>();
        vb[i] = __builtin_amdgcn_raw_buffer_load_b128(rv, voff, i * 32768, POL_V);
        __builtin_amdgcn_sched_barrier(0);
      }
      if (i == FSTART && sw == 0 && lane == 0) flags[F_VI] = 1u;
      if (i == PSTART) {
        STAMP(4);
        lds_wait(flags + F_P, 1u);
        STAMP(5);
#pragma unroll
        for (int u = 0; u < 4; ++u) pp[u] = *(const LAS v4u*)(L + L_P + (sw * 8 + (lane >> 3)) * 64 + u * 16);
      }
      if (i == 30) {
        lds_wait(flags + F_WO, 1u);
#pragma unroll
        for (int s = 0; s < 2; ++s) {
          bw[s] = bfrag(L + L_WO + (32 * s + tr_row) * (D * 2) + t0 * 32 + tr_col, D * 2);
          bw[2 + s] = bfrag(L + L_WO + (32 * s + tr_row) * (D * 2) + t1 * 32 + tr_col, D * 2);
          bw[4 + s] = v4u{0u, 0u, 0u, 0u};
        }
      }
      const int m0 = pairs_done(i - 1), m1 = i < 32 ? pairs_done(i) : 16;
#pragma unroll
      for (int m = m0; m < m1; ++m) {
        const unsigned pw = pp[m >> 2][m & 3];
#pragma unroll
        for (int j = 0; j < 4; ++j) {
          const unsigned a = vb[2 * m][j], bb = vb[2 * m + 1][j];
          acc[2 * j] = dot2(__builtin_amdgcn_perm(bb, a, 0x05040100u), pw, acc[2 * j]);
          acc[2 * j + 1] = dot2(__builtin_amdgcn_perm(bb, a, 0x07060302u), pw, acc[2 * j + 1]);
        }
      }
    }
    STAMP(6);
    float a4[4], a2[2];
#pragma unroll
    for (int u = 0; u < 4; ++u) a4[u] = tr32(acc[u], acc[u + 4]);
#pragma unroll
    for (int u = 0; u < 2; ++u) a2[u] = tr16(a4[u], a4[u + 2]);
    const float o1 = trd<ROR8>(a2[0], a2[1], lane & 8);
    const int dim = (lane & 7) * 8 + ((lane >> 5) & 1) * 4 + ((lane >> 4) & 1) * 2 + ((lane >> 3) & 1);
    const float oh = rbf(o1);
    const unsigned hl = pack2(oh, o1 - oh);
    *(LAS u16*)(L + L_OA + sw * 144 + dim * 2) = (u16)hl;
    *(LAS u16*)(L + L_OA + (8 + sw) * 144 + dim * 2) = (u16)(hl >> 16);
  } else {
    const int a = wave;
    const rsrc_t rx = mkrsrc(p.x, 2 * D * 2);
    const int l56 = lane < 56 ? lane : 55;
    v4u xa, xb; unsigned cv0, cv1, xd0, xd1;
    const int rq = (cu & 15) >> 2, cbq = cu & 3;
    const int qk0 = rq < 2 ? rq * 128 : 64 + rq * 96, qnr = rq < 2 ? 128 : 96;
    const int qr = 2 * lane < qnr ? 2 * lane : qnr - 2;
    if (is_mod) {
      const rsrc_t rc = mkrsrc(p.c, 2 * D * 2);
      cv0 = __builtin_amdgcn_raw_buffer_load_b32(rc, (a * 112 + 2 * l56) * 2, 0, 0);
      cv1 = __builtin_amdgcn_raw_buffer_load_b32(rc, (D + a * 112 + 2 * l56) * 2, 0, 0);
      const GAS unsigned char* src = (const GAS unsigned char*)p.wmod + (size_t)(a * 112 + (lane >> 3)) * (6 * D * 2) + mb * 128 + (lane & 7) * 16;
#pragma unroll
      for (int i = 0; i < 14; ++i)
        __builtin_amdgcn_global_load_lds((const GAS unsigned*)(src + (size_t)i * 8 * (6 * D * 2)), (LAS unsigned*)(L + L_WM + (a * 14 + i) * 1024), 16, 0, POL_W);
    }
    if (a == 0) {
      xa = __builtin_amdgcn_raw_buffer_load_b128(rx, l56 * 16, 0, 0);
      xb = __builtin_amdgcn_raw_buffer_load_b128(rx, D * 2 + l56 * 16, 0, 0);
      xd0 = __builtin_amdgcn_raw_buffer_load_b32(rx, (qk0 + qr) * 2, 0, 0);
      xd1 = __builtin_amdgcn_raw_buffer_load_b32(rx, (D + qk0 + qr) * 2, 0, 0);
      const GAS unsigned char* src = (const GAS unsigned char*)p.wq + (size_t)(qk0 + (lane >> 3)) * (NH * DH * 2) + (h * 256 + cbq * 64) * 2 + (lane & 7) * 16;
#pragma unroll
      for (int i = 0; i < 16; ++i)
        if (i < 12 || rq < 2)
          __builtin_amdgcn_global_load_lds((const GAS unsigned*)(src + (size_t)i * 8 * (NH * DH * 2)), (LAS unsigned*)(L + L_WQ + i * 1024), 16, 0, POL_W);
    }
    bar();
    T = __builtin_amdgcn_readfirstlane(e0) + 1u;
    if (is_mod) {
      {
        const float c00 = lo16(cv0), c01 = hi16(cv0), c10 = lo16(cv1), c11 = hi16(cv1);
        const float s00 = c00 / (1.f + __expf(-c00)), s01 = c01 / (1.f + __expf(-c01));
        const float s10 = c10 / (1.f + __expf(-c10)), s11 = c11 / (1.f + __expf(-c11));
        *(LAS unsigned*)(L + L_H2 + (a * 112 + 2 * l56) * 2) = pack2(s00, s01);
        *(LAS unsigned*)(L + L_H2 + D * 2 + (a * 112 + 2 * l56) * 2) = pack2(s10, s11);
      }
      waitvm<0>();
      waitlds();
      if (lane == 0) lds_inc(flags + F_MOD);
      lds_wait(flags + F_MOD, 4u);
      v4f da = {0.f, 0.f, 0.f, 0.f}, db = {0.f, 0.f, 0.f, 0.f};
      LAS unsigned char* mq = L + L_WM + tr_row * 128 + a * 32 + tr_col;
#pragma unroll
      for (int s = 0; s < 14; s += 2) {
        const v4u a0 = *(const LAS v4u*)(L + L_H2 + (lane & 1) * (D * 2) + s * 64 + g16 * 16);
        const v4u a1 = *(const LAS v4u*)(L + L_H2 + (lane & 1) * (D * 2) + s * 64 + 64 + g16 * 16);
        da = mfma(a0, bfrag(mq + s * 4096, 128), da); db = mfma(a1, bfrag(mq + s * 4096 + 4096, 128), db);
      }
      const unsigned g = pack2(da[0] + db[0], da[1] + db[1]), gn = dppu<XOR1>(g);
      if (lane < 16 && (lane & 1) == 0) publish(rs, WS_MOD + (mb * 32 + a * 8 + (lane >> 1)) * 16, g, gn, T);
      STAMP(1);
    }
    if (a == 0) {
      waitvm<0>();
      v4u bq[16];
      {
        LAS unsigned char* qq = L + L_WQ + tr_row * 128 + tr_col;
#pragma unroll
        for (int s = 0; s < 4; ++s)
#pragma unroll
          for (int t = 0; t < 4; ++t) bq[s * 4 + t] = bfrag(qq + (s < 3 || rq < 2 ? s : 0) * 4096 + t * 32, 128);
      }
      float sm0 = 0.f, sm1 = 0.f;
#pragma unroll
      for (int j = 0; j < 4; ++j) { sm0 += lo16(xa[j]) + hi16(xa[j]); sm1 += lo16(xb[j]) + hi16(xb[j]); }
      if (lane >= 56) { sm0 = 0.f; sm1 = 0.f; }
      const float mean0 = wave_sum(sm0) * (1.f / D), mean1 = wave_sum(sm1) * (1.f / D);
      float v0 = 0.f, v1 = 0.f;
#pragma unroll
      for (int j = 0; j < 4; ++j) {
        float d = lo16(xa[j]) - mean0; v0 = fmaf(d, d, v0); d = hi16(xa[j]) - mean0; v0 = fmaf(d, d, v0);
        d = lo16(xb[j]) - mean1; v1 = fmaf(d, d, v1); d = hi16(xb[j]) - mean1; v1 = fmaf(d, d, v1);
      }
      if (lane >= 56) { v0 = 0.f; v1 = 0.f; }
      const float rs0 = ln_rsd(wave_sum(v0) * (1.f / D)), rs1 = ln_rsd(wave_sum(v1) * (1.f / D));
      const float mb0 = rbf(mean0), mb1 = rbf(mean1);
      const float n00 = rbf(rbf(lo16(xd0) - mb0) * rs0), n01 = rbf(rbf(hi16(xd0) - mb0) * rs0);
      const float n10 = rbf(rbf(lo16(xd1) - mb1) * rs1), n11 = rbf(rbf(hi16(xd1) - mb1) * rs1);
      {
        int off[2] = {WS_MOD + ((qk0 + qr) >> 1) * 16, WS_MOD + (224 + ((qk0 + qr) >> 1)) * 16}; v4u g[2];
        gather<2, PSLP>(rs, off, g, T);
        STAMP(2);
        const float h00 = rbf(n00 * rbf(1.f + lo16(g[1].x))) + lo16(g[0].x), h10 = rbf(n10 * rbf(1.f + hi16(g[1].x))) + hi16(g[0].x);
        const float h01 = rbf(n01 * rbf(1.f + lo16(g[1].z))) + lo16(g[0].z), h11 = rbf(n11 * rbf(1.f + hi16(g[1].z))) + hi16(g[0].z);
        const bool live = 2 * lane < qnr;
        *(LAS unsigned*)(L + L_H1 + lane * 4) = live ? pack2(h00, h01) : 0u;
        *(LAS unsigned*)(L + L_H1 + 256 + lane * 4) = live ? pack2(h10, h11) : 0u;
      }
      {
        v4f d[4];
#pragma unroll
        for (int t = 0; t < 4; ++t) d[t] = v4f{0.f, 0.f, 0.f, 0.f};
#pragma unroll
        for (int s = 0; s < 4; ++s) {
          const v4u aa = *(const LAS v4u*)(L + L_H1 + (lane & 1) * 256 + s * 64 + g16 * 16);
#pragma unroll
          for (int t = 0; t < 4; ++t) d[t] = mfma(aa, bq[s * 4 + t], d[t]);
        }
        const float q0 = g16 == 0 ? d[0][0] : (g16 == 1 ? d[1][0] : (g16 == 2 ? d[2][0] : d[3][0]));
        const float q1 = g16 == 0 ? d[0][1] : (g16 == 1 ? d[1][1] : (g16 == 2 ? d[2][1] : d[3][1]));
        const bool odd = lane & 1;
        const float rcv = dpp<XOR1>(odd ? q0 : q1);
        publish(rs, WS_Q + ((((lane & 1) * 16 + h) * 4 + rq) * 128 + cbq * 32 + (lane >> 1)) * 16,
                __float_as_uint(odd ? rcv : q0), __float_as_uint(odd ? q1 : rcv), T);
      }
      STAMP(3);
      {
        int off[8]; v4u g[8];
#pragma unroll
        for (int k = 0; k < 8; ++k) off[k] = WS_Q + bh * 8192 + (k * 64 + lane) * 16;
        gather<8, PSLP>(rs, off, g, T);
#pragma unroll
        for (int u = 0; u < 2; ++u) {
          const float qa = (__uint_as_float(g[u].x) + __uint_as_float(g[2 + u].x)) + (__uint_as_float(g[4 + u].x) + __uint_as_float(g[6 + u].x));
          const float qb = (__uint_as_float(g[u].z) + __uint_as_float(g[2 + u].z)) + (__uint_as_float(g[4 + u].z) + __uint_as_float(g[6 + u].z));
          *(LAS unsigned*)(L + L_Q + (u * 64 + lane) * 4) = pack2(qa, qb);
        }
        waitlds();
        if (lane == 0) flags[F_Q] = 1u;
      }
      STAMP(4);
      lds_wait(flags + F_SC, 8u);
      {
        int off[16]; v4u g[16];
#pragma unroll
        for (int k = 0; k < 16; ++k) off[k] = WS_SC + bh * 16384 + (((k ^ (kh * 8)) * 64) + lane) * 16;
        gather<16, PSLP>(rs, off, g, T);
        STAMP(5);
        float m = -INFINITY;
#pragma unroll
        for (int k = 0; k < 16; ++k) m = fmaxf(fmaxf(fmaxf(lo16(g[k].x), hi16(g[k].x)), fmaxf(lo16(g[k].z), hi16(g[k].z))), m);
        m = wave_max(m);
        const float cs = 0.0625f * 1.4426950408889634f;
        const float mc = m * cs;
        float e[32]; float l = 0.f;
#pragma unroll
        for (int k = 0; k < 16; ++k) {
          const float e0_ = __builtin_amdgcn_exp2f(fmaf(lo16(g[k].x), cs, -mc)), e1_ = __builtin_amdgcn_exp2f(fmaf(hi16(g[k].x), cs, -mc));
          const float e2_ = __builtin_amdgcn_exp2f(fmaf(lo16(g[k].z), cs, -mc)), e3_ = __builtin_amdgcn_exp2f(fmaf(hi16(g[k].z), cs, -mc));
          l += (e0_ + e1_) + (e2_ + e3_);
          if (k < 8) { e[4 * k] = e0_; e[4 * k + 1] = e1_; e[4 * k + 2] = e2_; e[4 * k + 3] = e3_; }
        }
        l = wave_sum(l);
        const float rl = rcp_nr(l);
        const int t = lane & 15;
        const int half = t >> 3, j0 = (t >> 2) & 1, j1 = (t >> 1) & 1, j4 = t & 1;
#pragma unroll
        for (int k = 0; k < 8; ++k) {
          const int cq = k >> 1;
          const int wp = (k & 1) * 4 + (lane >> 4);
          const int wc = (j1 << 2) | (j0 << 1) | (wp >> 2), gg = ((wp & 3) << 1) | half, mm = (cq << 2) | (j4 << 1);
          *(LAS v2u*)(L + L_P + ((wc * 8 + gg) * 16 + mm) * 4) = v2u{pack2(e[4 * k] * rl, e[4 * k + 1] * rl), pack2(e[4 * k + 2] * rl, e[4 * k + 3] * rl)};
        }
        waitlds();
        if (lane == 0) flags[F_P] = 1u;
      }
      STAMP(6);
    } else if (a == 1) {
      lds_wait(flags + F_KI, 1u);
      const GAS unsigned char* src = (const GAS unsigned char*)p.wo + (size_t)(h * 256 + dq * 64) * (D * 2) + lane * 16;
#pragma unroll
      for (int i = 0; i < 56; ++i) {
        if (i >= WDEPTH) waitvm<WDEPTH - 1>();
        __builtin_amdgcn_global_load_lds((const GAS unsigned*)(src + i * 1024), (LAS unsigned*)(L + L_WO + i * 1024), 16, 0, POL_W);
      }
      waitvm<0>();
      if (lane == 0) flags[F_WO] = 1u;
      STAMP(2);
    } else if (a == 3) {
      if (is_red) {
        const unsigned xr = __builtin_amdgcn_raw_buffer_load_b32(rx, (rb * D + rchunk * 8 + (lane & 3) * 2) * 2, 0, 0);
        int off[2] = {WS_MOD + (D + rchunk * 4 + (lane & 3)) * 16, WS_MOD + (5 * D / 2 + rchunk * 4 + (lane & 3)) * 16}; v4u g[2];
        gather<2, 32>(rs, off, g, T);
        kp[0] = rb ? hi16(g[0].x) : lo16(g[0].x); kp[1] = rb ? hi16(g[0].z) : lo16(g[0].z);
        kp[2] = rb ? hi16(g[1].x) : lo16(g[1].x); kp[3] = rb ? hi16(g[1].z) : lo16(g[1].z);
        kp[4] = lo16(xr); kp[5] = hi16(xr);
        STAMP(2);
      }
    }
    if (is_ffn) {
      const int col2 = ((a & 1) * 56 + l56) * 2;
      int off[4] = {WS_MOD + (2 * D + col2) * 16, WS_MOD + (2 * D + col2 + 1) * 16, WS_MOD + (3 * D / 2 + col2) * 16, WS_MOD + (3 * D / 2 + col2 + 1) * 16};
      v4u g[4];
      gather<4, 32>(rs, off, g, T);
      const bool hb = a >> 1;
      kp[0] = rbf(1.f + (hb ? hi16(g[0].x) : lo16(g[0].x))); kp[1] = rbf(1.f + (hb ? hi16(g[0].z) : lo16(g[0].z)));
      kp[2] = rbf(1.f + (hb ? hi16(g[1].x) : lo16(g[1].x))); kp[3] = rbf(1.f + (hb ? hi16(g[1].z) : lo16(g[1].z)));
      kp[4] = hb ? hi16(g[2].x) : lo16(g[2].x); kp[5] = hb ? hi16(g[2].z) : lo16(g[2].z);
      kp[6] = hb ? hi16(g[3].x) : lo16(g[3].x); kp[7] = hb ? hi16(g[3].z) : lo16(g[3].z);
    }
    lds_wait(flags + F_WO, 1u);
#pragma unroll
    for (int s = 0; s < 2; ++s) {
      bw[s] = bfrag(L + L_WO + (32 * s + tr_row) * (D * 2) + t0 * 32 + tr_col, D * 2);
      bw[2 + s] = bfrag(L + L_WO + (32 * s + tr_row) * (D * 2) + t1 * 32 + tr_col, D * 2);
      bw[4 + s] = bfrag(L + L_WO + (32 * s + tr_row) * (D * 2) + t2 * 32 + tr_col, D * 2);
    }
    if (is_ffn && a >= 2) {
      lds_wait(flags + F_VI, 1u);
      const GAS unsigned char* gsrc = (const GAS unsigned char*)((lane & 4) ? p.wu : p.wg) + (size_t)((a - 2) * 224 + (lane >> 3)) * (F * 2) + fidx * 64 + (lane & 3) * 16;
#pragma unroll
      for (int i = 0; i < 28; ++i) {
        if (i >= FDEPTH) waitvm<FDEPTH - 1>();
        __builtin_amdgcn_global_load_lds((const GAS unsigned*)(gsrc + (size_t)i * 8 * (F * 2)), (LAS unsigned*)(L + L_GU + ((a - 2) * 28 + i) * 1024), 16, 0, POL_W);
      }
      const GAS unsigned char* dsrc = (const GAS unsigned char*)p.wd + (size_t)fidx * 32 * (D * 2) + (a - 2) * 14336 + lane * 16;
#pragma unroll
      for (int i = 0; i < 14; ++i) {
        waitvm<FDEPTH - 1>();
        __builtin_amdgcn_global_load_lds((const GAS unsigned*)(dsrc + i * 1024), (LAS unsigned*)(L + L_DN + (a - 2) * 14336 + i * 1024), 16, 0, POL_W);
      }
    }
    STAMP(7);
  }

  bar();
  const int n16 = lane & 15;
  const int tsel = g16 == 0 ? t0 : (g16 == 2 ? t1 : t2);
  {
    const v4u a0 = *(const LAS v4u*)(L + L_OA + n16 * 144 + g16 * 16), a1 = *(const LAS v4u*)(L + L_OA + n16 * 144 + 64 + g16 * 16);
    const v4f z = {0.f, 0.f, 0.f, 0.f};
    const v4f d0 = mfma(a1, bw[1], mfma(a0, bw[0], z)), d1 = mfma(a1, bw[3], mfma(a0, bw[2], z)), d2 = mfma(a1, bw[5], mfma(a0, bw[4], z));
    const float u1 = tr32((d0[0] + d0[1]) + (d0[2] + d0[3]), (d1[0] + d1[1]) + (d1[2] + d1[3]));
    const float u2 = sum32((d2[0] + d2[1]) + (d2[2] + d2[3]));
    const float v = tr16(u1, u2);
    const float vn = dpp<XOR1>(v);
    const int n = tsel * 16 + n16;
    if ((lane & 1) == 0 && ((g16 & 1) == 0 || (g16 == 1 && wave < 4)))
      publish(rs, WS_P1 + (((b * 56 + (n >> 3)) * 128 + h * 8 + c) * 4 + ((n >> 1) & 3)) * 16, __float_as_uint(v), __float_as_uint(vn), T);
    STAMP(8);
  }
  if (is_red) {
    if (wave == 3) {
      float x20, x21;
      {
        int off[8]; v4u g[8];
#pragma unroll
        for (int k = 0; k < 8; ++k) off[k] = WS_P1 + (rb * 56 + rchunk) * 8192 + (k * 64 + lane) * 16;
        gather<8, 1>(rs, off, g, T);
        STAMP(9);
        float s0 = 0.f, s1 = 0.f;
#pragma unroll
        for (int k = 0; k < 8; k += 2) { s0 += __uint_as_float(g[k].x) + __uint_as_float(g[k + 1].x); s1 += __uint_as_float(g[k].z) + __uint_as_float(g[k + 1].z); }
        s0 += dpp<ROR4>(s0); s1 += dpp<ROR4>(s1); s0 += dpp<ROR8>(s0); s1 += dpp<ROR8>(s1);
        s0 = sum16(s0); s1 = sum16(s1); s0 = sum32(s0); s1 = sum32(s1);
        x20 = rbf(kp[4] + rbf(kp[0] * rbf(s0))); x21 = rbf(kp[5] + rbf(kp[1] * rbf(s1)));
        const unsigned xp = pack2(x20, x21), xq = dppu<XOR1>(xp);
        float sm = x20 + x21; sm += dpp<XOR1>(sm); sm += dpp<XOR2>(sm);
        const float d0 = x20 - sm * 0.125f, d1 = x21 - sm * 0.125f;
        float m2 = fmaf(d0, d0, d1 * d1); m2 += dpp<XOR1>(m2); m2 += dpp<XOR2>(m2);
        const bool stl = lane == 1;
        if (lane < 3) publish(rs, stl ? WS_ST + (rb * 56 + rchunk) * 16 : WS_X2 + (rb * 112 + rchunk * 2 + (lane >> 1)) * 16,
                              stl ? __float_as_uint(sm) : xp, stl ? __float_as_uint(m2) : xq, T);
        STAMP(10);
      }
      {
        int off[4]; v4u g[4];
#pragma unroll
        for (int k = 0; k < 4; ++k) { int pc = k * 64 + lane; pc = pc < 224 ? pc : pc - 32; off[k] = WS_P2 + (rb * 56 + rchunk) * (NF * 64) + pc * 16; }
        gather<4, 1>(rs, off, g, T);
        float s0 = (__uint_as_float(g[0].x) + __uint_as_float(g[1].x)) + (__uint_as_float(g[2].x) + (lane < 32 ? __uint_as_float(g[3].x) : 0.f));
        float s1 = (__uint_as_float(g[0].z) + __uint_as_float(g[1].z)) + (__uint_as_float(g[2].z) + (lane < 32 ? __uint_as_float(g[3].z) : 0.f));
        s0 += dpp<ROR4>(s0); s1 += dpp<ROR4>(s1); s0 += dpp<ROR8>(s0); s1 += dpp<ROR8>(s1);
        s0 = sum16(s0); s1 = sum16(s1); s0 = sum32(s0); s1 = sum32(s1);
        const unsigned ov = pack2(x20 + rbf(kp[2] * rbf(s0)), x21 + rbf(kp[3] * rbf(s1)));
        if (lane < 4) ((GAS unsigned*)p.out)[(rb * D + rchunk * 8) / 2 + lane] = ov;
        if (bid == 13 * 8 && lane == 0) __builtin_amdgcn_raw_buffer_store_b32(T, rs, WS_EPOCH, 0, AUX_SC1);
        STAMP(11);
      }
    }
  } else if (is_ffn) {
    if (wave == 2 || wave == 3) { waitvm<0>(); if (lane == 0) lds_inc(flags + F_FW); STAMP(1); }
    lds_wait(flags + F_FW, 2u);
    v4u bd[3];
    bd[0] = bfrag(L + L_DN + tr_row * (D * 2) + t0 * 32 + tr_col, D * 2);
    bd[1] = bfrag(L + L_DN + tr_row * (D * 2) + t1 * 32 + tr_col, D * 2);
    bd[2] = bfrag(L + L_DN + tr_row * (D * 2) + t2 * 32 + tr_col, D * 2);
    if (wave < 4) {
      v4u bg[14];
      {
        LAS unsigned char* gq = L + L_GU + tr_row * 128 + (lane & 2) * 32 + wave * 16 + (lane & 1) * 8;
#pragma unroll
        for (int s = 0; s < 14; ++s) bg[s] = bfrag(gq + s * 32 * 128, 128);
      }
      const int l56 = lane < 56 ? lane : 55, xbt = wave >> 1;
      {
        int off[2] = {WS_X2 + (wave * 56 + l56) * 16, WS_ST + (xbt * 56 + l56) * 16}; v4u g[2];
        gather<2, 1>(rs, off, g, T);
        STAMP(9);
        const float sm = lane < 56 ? __uint_as_float(g[1].x) : 0.f;
        const float mean = wave_sum(sm) * (1.f / D);
        const float dm = sm * 0.125f - mean;
        const float m2 = wave_sum(lane < 56 ? fmaf(8.f * dm, dm, __uint_as_float(g[1].z)) : 0.f);
        const float rsd = ln_rsd(m2 * (1.f / D)), mbf = rbf(mean);
        const float h0 = rbf(rbf(rbf(lo16(g[0].x) - mbf) * rsd) * kp[0]) + kp[4], h1 = rbf(rbf(rbf(hi16(g[0].x) - mbf) * rsd) * kp[1]) + kp[5];
        const float h2 = rbf(rbf(rbf(lo16(g[0].z) - mbf) * rsd) * kp[2]) + kp[6], h3 = rbf(rbf(rbf(hi16(g[0].z) - mbf) * rsd) * kp[3]) + kp[7];
        *(LAS v2u*)(L + L_H2 + (wave * 56 + l56) * 8) = v2u{pack2(h0, h1), pack2(h2, h3)};
      }
      bar();
      v4f da = {0.f, 0.f, 0.f, 0.f}, db = {0.f, 0.f, 0.f, 0.f};
#pragma unroll
      for (int s = 0; s < 14; s += 2) {
        const v4u a0 = *(const LAS v4u*)(L + L_H2 + (lane & 1) * (D * 2) + s * 64 + g16 * 16);
        const v4u a1 = *(const LAS v4u*)(L + L_H2 + (lane & 1) * (D * 2) + s * 64 + 64 + g16 * 16);
        da = mfma(a0, bg[s], da); db = mfma(a1, bg[s + 1], db);
      }
      const float g0 = rbf(da[0] + db[0]), g1 = rbf(da[1] + db[1]);
      const float u0 = dpp<ROR8>(g0), u1 = dpp<ROR8>(g1);
      const float k1 = -2.f * 0.7978845608028654f * 1.4426950408889634f;
      const float e0_ = __builtin_amdgcn_exp2f(k1 * fmaf(0.044715f * g0 * g0, g0, g0)), e1_ = __builtin_amdgcn_exp2f(k1 * fmaf(0.044715f * g1 * g1, g1, g1));
      const float ge0 = rbf(g0 * __builtin_amdgcn_rcpf(1.f + e0_)), ge1 = rbf(g1 * __builtin_amdgcn_rcpf(1.f + e1_));
      const unsigned av = pack2(ge0 * u0, ge1 * u1);
      if (lane < 8) {
        *(LAS u16*)(L + L_ACT + (wave * 8 + lane) * 2) = (u16)av;
        *(LAS u16*)(L + L_ACT + 64 + (wave * 8 + lane) * 2) = (u16)(av >> 16);
      }
      STAMP(10);
    } else {
      bar();
    }
    bar();
    {
      const v4u aa = *(const LAS v4u*)(L + L_ACT + (lane & 1) * 64 + g16 * 16);
      const v4f z = {0.f, 0.f, 0.f, 0.f};
      const v4f d0 = mfma(aa, bd[0], z), d1 = mfma(aa, bd[1], z), d2 = mfma(aa, bd[2], z);
      const float v0 = g16 == 0 ? d0[0] : (g16 == 2 ? d1[0] : d2[0]), v1 = g16 == 0 ? d0[1] : (g16 == 2 ? d1[1] : d2[1]);
      const bool odd = lane & 1;
      const float rcv = dpp<XOR1>(odd ? v0 : v1);
      const int n = tsel * 16 + n16;
      if ((g16 & 1) == 0 || (g16 == 1 && wave < 4))
        publish(rs, WS_P2 + ((((lane & 1) * 56 + (n >> 3)) * NF + fidx) * 4 + ((n >> 1) & 3)) * 16,
                __float_as_uint(odd ? rcv : v0), __float_as_uint(odd ? v1 : rcv), T);
      STAMP(11);
    }
  }
  STAMP_FLUSH();
}
}

extern "C" void kernel_launch(void* const* d_in, const int* in_sizes, int n_in, void* d_out, int out_size, void* d_ws,
                              size_t ws_size, hipStream_t stream) {
  Params p;
  p.x = (const u16*)d_in[0]; p.kc = (const u16*)d_in[1]; p.vc = (const u16*)d_in[2];
  p.wq = (const u16*)d_in[3]; p.wo = (const u16*)d_in[4];
  p.wg = (const u16*)d_in[6]; p.wu = (const u16*)d_in[7]; p.wd = (const u16*)d_in[8];
  p.c = (const u16*)d_in[9]; p.wmod = (const u16*)d_in[10];
  p.out = (u16*)d_out; p.ws = (unsigned char*)d_ws;
  (void)hipFuncSetAttribute((const void*)fused_block, hipFuncAttributeMaxDynamicSharedMemorySize, L_END);
  hipLaunchKernelGGL(fused_block, dim3(256), dim3(NTHR), L_END, stream, p);
}
```

```cpp
#include <hip/hip_runtime.h>
#include <stdint.h>

typedef unsigned short u16;
typedef unsigned v4u __attribute__((ext_vector_type(4)));
typedef unsigned v2u __attribute__((ext_vector_type(2)));
typedef float v4f __attribute__((ext_vector_type(4)));
typedef float v2f __attribute__((ext_vector_type(2)));
typedef short v8s __attribute__((ext_vector_type(8)));
typedef short v4s __attribute__((ext_vector_type(4)));
typedef __bf16 bf16x2 __attribute__((ext_vector_type(2)));
#define GAS __attribute__((address_space(1)))
#define LAS __attribute__((address_space(3)))

#define KDEPTH 3
#define VDEPTH 3
#define WDEPTH 8
#define PSTART 24
#define G_MOD 400
#define G_Q 150
#define G_SC 100
#define TBIG 16
#define TTHR 64
#define PTHR 38
#define HOLDK 14
#define PSLP 24
#define POL_K 2
#define POL_V 2
#define POL_W 0
#define DBG_STAMP 0

namespace {
constexpr int D = 448, NH = 16, SK = 4096, DH = 256, F = 1792;
constexpr int NTHR = 768;
constexpr int NF = 56;
constexpr int AUX_NT = 2, AUX_SC1 = 16;
constexpr unsigned SPIN_CAP = 1u << 16;

constexpr int WS_EPOCH = 0;
constexpr int WS_MOD = 256;
constexpr int WS_X2 = 22528;
constexpr int WS_ST = 26624;
constexpr int WS_Q = 32768;
constexpr int WS_SC = WS_Q + 32 * 8192;
constexpr int WS_P1 = WS_SC + 32 * 16384;
constexpr int WS_P2 = WS_P1 + 2 * 56 * 8192;
constexpr int WS_END = WS_P2 + 2 * 56 * NF * 64;
constexpr int WS_STAMP = 4 << 20;
static_assert(WS_END <= WS_STAMP, "ws map");

constexpr int L_WO = 0;
constexpr int L_GU = 57344;
constexpr int L_DN = L_GU + 57344;
constexpr int L_Q = L_DN + 28672;
constexpr int L_H1 = L_Q + 512;
constexpr int L_P = L_H1 + 512;
constexpr int L_OA = L_P + 4096;
constexpr int L_H2 = L_OA + 2304;
constexpr int L_WM = L_GU;
constexpr int L_WQ = L_DN;
constexpr int L_ACT = L_H2 + 1792;
constexpr int L_FLAG = L_ACT + 128;
constexpr int L_LAT = L_FLAG + 64;
constexpr int L_END = L_LAT + 32;
static_assert(L_END <= 160 * 1024 && L_END > 80 * 1024, "lds");
enum { F_Q = 0, F_P = 1, F_WO = 2, F_SC = 3, F_MOD = 4, F_KI = 5, F_TV = 6, F_T = 8 };

struct Params {
  const u16 *x, *kc, *vc, *wq, *wo, *wg, *wu, *wd, *c, *wmod;
  u16* out;
  unsigned char* ws;
};

__device__ __forceinline__ float lo16(unsigned w) { return __uint_as_float(w << 16); }
__device__ __forceinline__ float hi16(unsigned w) { return __uint_as_float(w & 0xffff0000u); }
__device__ __forceinline__ unsigned pack2(float a, float b) {
  bf16x2 v = {(__bf16)a, (__bf16)b};
  return __builtin_bit_cast(unsigned, v);
}
__device__ __forceinline__ float rbf(float f) { return lo16(pack2(f, f)); }
__device__ __forceinline__ float dot2(unsigned a, unsigned b, float c) {
  return __builtin_amdgcn_fdot2_f32_bf16(__builtin_bit_cast(bf16x2, a), __builtin_bit_cast(bf16x2, b), c, false);
}
__device__ __forceinline__ float rcp_nr(float x) { const float r = __builtin_amdgcn_rcpf(x); return fmaf(fmaf(-x, r, 1.f), r, r); }
template <int N> __device__ __forceinline__ void waitvm() {
  asm volatile("" ::: "memory");
  __builtin_amdgcn_s_waitcnt((N & 15) | ((N >> 4) << 14) | 0x0F70);
  asm volatile("" ::: "memory");
}
__device__ __forceinline__ void waitlds() { asm volatile("s_waitcnt lgkmcnt(0)" ::: "memory"); }
__device__ __forceinline__ void bar() {
  __builtin_amdgcn_sched_barrier(0);
  asm volatile("s_waitcnt lgkmcnt(0)" ::: "memory");
  __builtin_amdgcn_s_barrier();
  asm volatile("" ::: "memory");
  __builtin_amdgcn_sched_barrier(0);
}
template <int CTRL> __device__ __forceinline__ float dpp(float x) {
  return __builtin_bit_cast(float, __builtin_amdgcn_mov_dpp(__builtin_bit_cast(int, x), CTRL, 0xf, 0xf, true));
}
template <int CTRL> __device__ __forceinline__ unsigned dppu(unsigned x) {
  return (unsigned)__builtin_amdgcn_mov_dpp((int)x, CTRL, 0xf, 0xf, true);
}
constexpr int XOR1 = 0xB1, XOR2 = 0x4E, XOR7 = 0x141, ROR8 = 0x128, ROR4 = 0x124;
__device__ __forceinline__ float tr32(float a, float b) {
  auto r = __builtin_amdgcn_permlane32_swap(__float_as_uint(a), __float_as_uint(b), false, false);
  return __uint_as_float(r[0]) + __uint_as_float(r[1]);
}
__device__ __forceinline__ float tr16(float a, float b) {
  auto r = __builtin_amdgcn_permlane16_swap(__float_as_uint(a), __float_as_uint(b), false, false);
  return __uint_as_float(r[0]) + __uint_as_float(r[1]);
}
template <int CTRL> __device__ __forceinline__ float trd(float a, float b, bool bit) {
  const float t1 = a + dpp<CTRL>(a), t2 = b + dpp<CTRL>(b);
  return bit ? t2 : t1;
}
__device__ __forceinline__ float sum32(float a) { return tr32(a, a); }
__device__ __forceinline__ float sum16(float a) { return tr16(a, a); }
__device__ __forceinline__ float wave_sum(float v) {
  v += dpp<XOR1>(v); v += dpp<XOR2>(v); v += dpp<XOR7>(v); v += dpp<ROR8>(v);
  v = sum16(v); v = sum32(v);
  return v;
}
__device__ __forceinline__ float wave_max(float v) {
  v = fmaxf(v, dpp<XOR1>(v)); v = fmaxf(v, dpp<XOR2>(v)); v = fmaxf(v, dpp<XOR7>(v)); v = fmaxf(v, dpp<ROR8>(v));
  auto r = __builtin_amdgcn_permlane16_swap(__float_as_uint(v), __float_as_uint(v), false, false);
  v = fmaxf(__uint_as_float(r[0]), __uint_as_float(r[1]));
  auto t = __builtin_amdgcn_permlane32_swap(__float_as_uint(v), __float_as_uint(v), false, false);
  return fmaxf(__uint_as_float(t[0]), __uint_as_float(t[1]));
}
__device__ __forceinline__ v4f mfma(v4u a, v4u b, v4f c) {
  return __builtin_amdgcn_mfma_f32_16x16x32_bf16(__builtin_bit_cast(v8s, a), __builtin_bit_cast(v8s, b), c, 0, 0, 0);
}
__device__ __forceinline__ v4u bfrag(LAS unsigned char* q, int stride) {
  const v2u a = __builtin_bit_cast(v2u, __builtin_amdgcn_ds_read_tr16_b64_v4i16((LAS v4s*)q));
  const v2u b = __builtin_bit_cast(v2u, __builtin_amdgcn_ds_read_tr16_b64_v4i16((LAS v4s*)(q + 4 * stride)));
  return v4u{a.x, a.y, b.x, b.y};
}

typedef __amdgpu_buffer_rsrc_t rsrc_t;
__device__ __forceinline__ v4u ldstream(rsrc_t r, int voff, int soff, bool keep) {
  if (keep) return __builtin_amdgcn_raw_buffer_load_b128(r, voff, soff, 0);
  return __builtin_amdgcn_raw_buffer_load_b128(r, voff, soff, AUX_NT);
}
__device__ __forceinline__ rsrc_t mkrsrc(const void* p, int bytes) { return __builtin_amdgcn_make_buffer_rsrc((void*)p, 0, bytes, 0x00020000); }
__device__ __forceinline__ void publish(rsrc_t rs, int off, unsigned a, unsigned b, unsigned T) {
  v4u v = {a, T, b, T};
  __builtin_amdgcn_raw_buffer_store_b128(v, rs, off, 0, AUX_SC1);
}
template <int N, int SLP> __device__ __forceinline__ void gather(rsrc_t rs, const int (&off)[N], v4u (&v)[N], unsigned T) {
#pragma unroll
  for (int k = 0; k < N; ++k) v[k] = __builtin_amdgcn_raw_buffer_load_b128(rs, off[k], 0, AUX_SC1);
  for (unsigned spins = 0; spins < SPIN_CAP; ++spins) {
    bool ok = true;
#pragma unroll
    for (int k = 0; k < N; ++k) ok &= (v[k].y == T) & (v[k].w == T);
    const int miss = __builtin_popcountll(__builtin_amdgcn_ballot_w64(!ok));
    if (miss == 0) break;
    if (SLP < TBIG && miss > TTHR) __builtin_amdgcn_s_sleep(TBIG); else __builtin_amdgcn_s_sleep(SLP);
    asm volatile("" ::: "memory");
#pragma unroll
    for (int k = 0; k < N; ++k)
      if ((v[k].y != T) | (v[k].w != T)) v[k] = __builtin_amdgcn_raw_buffer_load_b128(rs, off[k], 0, AUX_SC1);
  }
}
__device__ __forceinline__ void lds_wait(volatile LAS unsigned* f, unsigned want) {
  for (unsigned spins = 0; spins < (1u << 22); ++spins) {
    if (__builtin_amdgcn_readfirstlane(*f) >= want) break;
    __builtin_amdgcn_s_sleep(1);
  }
  asm volatile("" ::: "memory");
}
__device__ __forceinline__ unsigned now() { return (unsigned)__builtin_amdgcn_s_memrealtime(); }
__device__ __forceinline__ void nap_until(unsigned t0, unsigned ticks) {
  for (int i = 0; i < 4096 && now() - t0 < ticks; ++i) __builtin_amdgcn_s_sleep(4);
}
__device__ __forceinline__ void lds_inc(volatile LAS unsigned* f) {
  __hip_atomic_fetch_add((LAS unsigned*)f, 1u, __ATOMIC_RELAXED, __HIP_MEMORY_SCOPE_WORKGROUP);
}
constexpr int pairs_done(int i) {
  if (i < PSTART) return 0;
  int a = (i - VDEPTH + 1) / 2, b = (i - PSTART + 1) * 3;
  return a < b ? a : b;
}
__device__ __forceinline__ float ln_rsd(float var) { return rcp_nr(rbf(__builtin_amdgcn_sqrtf(rbf(rbf(var) + 1e-5f)))); }

#define STAMP(i)
#define STAMP_FLUSH()

__global__ void __launch_bounds__(NTHR, 3) fused_block(Params p) {
  extern __shared__ __attribute__((aligned(16))) unsigned char smem[];
  LAS unsigned char* L = (LAS unsigned char*)smem;
  volatile LAS unsigned* flags = (volatile LAS unsigned*)(L + L_FLAG);
  const int bid = blockIdx.x, tid = threadIdx.x, lane = tid & 63;
  const int wave = __builtin_amdgcn_readfirstlane(tid >> 6);
  const int xcd = bid & 7, li = bid >> 3;
  const int cu = xcd * 32 + li;
  const int h = cu >> 4, b = (cu >> 3) & 1, c = cu & 7, kh = c >> 2, dq = c & 3, bh = b * 16 + h;
  const int mb = li * 8 + xcd;
  const bool is_mod = mb < 42, is_ffn = li >= 6 && li < 13, is_red = li >= 13 && li < 27;
  const int fidx = xcd * 7 + li - 6;
  const int rb = (li - 13) & 1, rchunk = xcd * 7 + ((li - 13) >> 1);
  const rsrc_t rs = mkrsrc(p.ws, WS_END);
  STAMP(0);
  const unsigned tstart = now();
  unsigned e0 = 0u;
  if (wave == 0) e0 = __builtin_amdgcn_raw_buffer_load_b32(rs, WS_EPOCH, 0, AUX_SC1);
  if (tid < 8) flags[tid] = 0u;
  unsigned T;
  v4u bw[6];
  float kp[8];
#pragma unroll
  for (int j = 0; j < 8; ++j) kp[j] = 0.f;
  const int t0 = wave < 4 ? 16 + wave : wave - 4, t1 = wave < 4 ? 20 + wave : wave + 4, t2 = wave < 4 ? 24 + wave : 0;
  const int tr_row = 8 * (lane >> 4) + ((lane >> 2) & 3), tr_col = (lane & 3) * 8, g16 = lane >> 4;

  if (wave >= 4) {
    const int sw = wave - 4;
    const rsrc_t rk = mkrsrc(p.kc, 64 << 20), rv = mkrsrc(p.vc, 64 << 20);
    const int koff = (bh * SK + c * 512) * 512 + sw * 1024 + lane * 16;
    const int voff = (bh * SK + kh * 2048 + sw * 8 + (lane >> 3)) * 512 + dq * 128 + (lane & 7) * 16;
    v4u kb[32], vb[32];
    constexpr bool keepk = false, keepv = false;
    bar();
    if (bid < HOLDK) lds_wait(flags + F_MOD, 4u);
#pragma unroll
    for (int i = 0; i < 32; ++i) {
      if (i >= KDEPTH) waitvm<KDEPTH - 1>();
      kb[i] = ldstream(rk, koff, i * 8192, keepk);
      __builtin_amdgcn_sched_barrier(0);
    }
    if (sw == 0 && lane == 0) flags[F_KI] = 1u;
    STAMP(1);
    lds_wait(flags + F_Q, 1u);
    STAMP(2);
    const v4u qv = *(const LAS v4u*)(L + L_Q + (lane & 31) * 16);
    float s2[16];
#pragma unroll
    for (int m = 0; m < 16; ++m) {
      float a = 0.f, bb = 0.f;
#pragma unroll
      for (int j = 0; j < 4; ++j) { a = dot2(kb[2 * m][j], qv[j], a); bb = dot2(kb[2 * m + 1][j], qv[j], bb); }
      s2[m] = tr16(a, bb);
      if ((m & 3) == 3) {
        __builtin_amdgcn_sched_barrier(0);
        waitvm<VDEPTH - 1>();
        vb[m >> 2] = ldstream(rv, voff, (m >> 2) * 32768, keepv);
        __builtin_amdgcn_sched_barrier(0);
      }
    }
    float s4[8], s8[4], s16[2];
#pragma unroll
    for (int u = 0; u < 8; ++u) s4[u] = trd<ROR8>(s2[2 * u], s2[2 * u + 1], lane & 8);
#pragma unroll
    for (int u = 0; u < 4; ++u) s8[u] = trd<XOR7>(s4[u], s4[u + 4], lane & 4);
#pragma unroll
    for (int u = 0; u < 2; ++u) s16[u] = trd<XOR2>(s8[u], s8[u + 2], lane & 2);
    const float sc = trd<XOR1>(s16[0], s16[1], lane & 1);
    T = __builtin_amdgcn_readfirstlane(flags[F_T]);
    {
      const unsigned pr = pack2(sc, dpp<XOR1>(sc));
      const unsigned pr2 = dppu<XOR2>(pr);
      if ((lane & 3) == 0) publish(rs, WS_SC + bh * 16384 + ((c * 8 + sw) * 16 + (lane >> 2)) * 16, pr, pr2, T);
      if (lane == 0) lds_inc(flags + F_SC);
    }
    STAMP(3);
    float acc[8];
#pragma unroll
    for (int j = 0; j < 8; ++j) acc[j] = 0.f;
    v4u pp[4];
#pragma unroll
    for (int i = 4; i <= 32; ++i) {
      if (i < 32) {
        __builtin_amdgcn_sched_barrier(0);
        waitvm<VDEPTH - 1>();
        vb[i] = ldstream(rv, voff, i * 32768, keepv);
        __builtin_amdgcn_sched_barrier(0);
      }
      if (i == PSTART) {
        STAMP(4);
        lds_wait(flags + F_P, 1u);
        STAMP(5);
#pragma unroll
        for (int u = 0; u < 4; ++u) pp[u] = *(const LAS v4u*)(L + L_P + (sw * 8 + (lane >> 3)) * 64 + u * 16);
      }
      if (i == 30) {
        lds_wait(flags + F_WO, 1u);
#pragma unroll
        for (int s = 0; s < 2; ++s) {
          bw[s] = bfrag(L + L_WO + (32 * s + tr_row) * (D * 2) + t0 * 32 + tr_col, D * 2);
          bw[2 + s] = bfrag(L + L_WO + (32 * s + tr_row) * (D * 2) + t1 * 32 + tr_col, D * 2);
          bw[4 + s] = v4u{0u, 0u, 0u, 0u};
        }
      }
      const int m0 = pairs_done(i - 1), m1 = i < 32 ? pairs_done(i) : 16;
#pragma unroll
      for (int m = m0; m < m1; ++m) {
        const unsigned pw = pp[m >> 2][m & 3];
#pragma unroll
        for (int j = 0; j < 4; ++j) {
          const unsigned a = vb[2 * m][j], bb = vb[2 * m + 1][j];
          acc[2 * j] = dot2(__builtin_amdgcn_perm(bb, a, 0x05040100u), pw, acc[2 * j]);
          acc[2 * j + 1] = dot2(__builtin_amdgcn_perm(bb, a, 0x07060302u), pw, acc[2 * j + 1]);
        }
      }
    }
    STAMP(6);
    float a4[4], a2[2];
#pragma unroll
    for (int u = 0; u < 4; ++u) a4[u] = tr32(acc[u], acc[u + 4]);
#pragma unroll
    for (int u = 0; u < 2; ++u) a2[u] = tr16(a4[u], a4[u + 2]);
    const float o1 = trd<ROR8>(a2[0], a2[1], lane & 8);
    const int dim = (lane & 7) * 8 + ((lane >> 5) & 1) * 4 + ((lane >> 4) & 1) * 2 + ((lane >> 3) & 1);
    const float oh = rbf(o1);
    const unsigned hl = pack2(oh, o1 - oh);
    *(LAS u16*)(L + L_OA + sw * 144 + dim * 2) = (u16)hl;
    *(LAS u16*)(L + L_OA + (8 + sw) * 144 + dim * 2) = (u16)(hl >> 16);
  } else {
    const int a = wave;
    const rsrc_t rx = mkrsrc(p.x, 2 * D * 2);
    const int l56 = lane < 56 ? lane : 55;
    v4u xa, xb; unsigned cv0, cv1, xd0, xd1;
    const int rq = (cu & 15) >> 2, cbq = cu & 3;
    const int qk0 = rq < 2 ? rq * 128 : 64 + rq * 96, qnr = rq < 2 ? 128 : 96;
    const int qr = 2 * lane < qnr ? 2 * lane : qnr - 2;
    if (is_mod) {
      const rsrc_t rc = mkrsrc(p.c, 2 * D * 2);
      cv0 = __builtin_amdgcn_raw_buffer_load_b32(rc, (a * 112 + 2 * l56) * 2, 0, 0);
      cv1 = __builtin_amdgcn_raw_buffer_load_b32(rc, (D + a * 112 + 2 * l56) * 2, 0, 0);
      const GAS unsigned char* src = (const GAS unsigned char*)p.wmod + (size_t)(a * 112 + (lane >> 3)) * (6 * D * 2) + mb * 128 + (lane & 7) * 16;
#pragma unroll
      for (int i = 0; i < 14; ++i)
        __builtin_amdgcn_global_load_lds((const GAS unsigned*)(src + (size_t)i * 8 * (6 * D * 2)), (LAS unsigned*)(L + L_WM + (a * 14 + i) * 1024), 16, 0, POL_W);
    }
    if (a == 0) {
      xa = __builtin_amdgcn_raw_buffer_load_b128(rx, l56 * 16, 0, 0);
      xb = __builtin_amdgcn_raw_buffer_load_b128(rx, D * 2 + l56 * 16, 0, 0);
      xd0 = __builtin_amdgcn_raw_buffer_load_b32(rx, (qk0 + qr) * 2, 0, 0);
      xd1 = __builtin_amdgcn_raw_buffer_load_b32(rx, (D + qk0 + qr) * 2, 0, 0);
      const GAS unsigned char* src = (const GAS unsigned char*)p.wq + (size_t)(qk0 + (lane >> 3)) * (NH * DH * 2) + (h * 256 + cbq * 64) * 2 + (lane & 7) * 16;
#pragma unroll
      for (int i = 0; i < 16; ++i)
        if (i < 12 || rq < 2)
          __builtin_amdgcn_global_load_lds((const GAS unsigned*)(src + (size_t)i * 8 * (NH * DH * 2)), (LAS unsigned*)(L + L_WQ + i * 1024), 16, 0, POL_W);
    }
    bar();
    if (a == 0) {
      T = __builtin_amdgcn_readfirstlane(e0) + 1u;
      if (lane == 0) { flags[F_T] = T; waitlds(); flags[F_TV] = 1u; }
    } else {
      lds_wait(flags + F_TV, 1u);
      T = __builtin_amdgcn_readfirstlane(flags[F_T]);
    }
    if (is_mod) {
      {
        const float c00 = lo16(cv0), c01 = hi16(cv0), c10 = lo16(cv1), c11 = hi16(cv1);
        const float s00 = c00 / (1.f + __expf(-c00)), s01 = c01 / (1.f + __expf(-c01));
        const float s10 = c10 / (1.f + __expf(-c10)), s11 = c11 / (1.f + __expf(-c11));
        *(LAS unsigned*)(L + L_H2 + (a * 112 + 2 * l56) * 2) = pack2(s00, s01);
        *(LAS unsigned*)(L + L_H2 + D * 2 + (a * 112 + 2 * l56) * 2) = pack2(s10, s11);
      }
      waitvm<0>();
      waitlds();
      if (lane == 0) lds_inc(flags + F_MOD);
      lds_wait(flags + F_MOD, 4u);
      v4f da = {0.f, 0.f, 0.f, 0.f}, db = {0.f, 0.f, 0.f, 0.f};
      LAS unsigned char* mq = L + L_WM + tr_row * 128 + a * 32 + tr_col;
#pragma unroll
      for (int s = 0; s < 14; s += 2) {
        const v4u a0 = *(const LAS v4u*)(L + L_H2 + (lane & 1) * (D * 2) + s * 64 + g16 * 16);
        const v4u a1 = *(const LAS v4u*)(L + L_H2 + (lane & 1) * (D * 2) + s * 64 + 64 + g16 * 16);
        da = mfma(a0, bfrag(mq + s * 4096, 128), da); db = mfma(a1, bfrag(mq + s * 4096 + 4096, 128), db);
      }
      const unsigned g = pack2(da[0] + db[0], da[1] + db[1]), gn = dppu<XOR1>(g);
      if (lane < 16 && (lane & 1) == 0) publish(rs, WS_MOD + (mb * 32 + a * 8 + (lane >> 1)) * 16, g, gn, T);
      STAMP(1);
    }
    if (a == 0) {
      waitvm<0>();
      v4u bq[16];
      {
        LAS unsigned char* qq = L + L_WQ + tr_row * 128 + tr_col;
#pragma unroll
        for (int s = 0; s < 4; ++s)
#pragma unroll
          for (int t = 0; t < 4; ++t) bq[s * 4 + t] = bfrag(qq + (s < 3 || rq < 2 ? s : 0) * 4096 + t * 32, 128);
      }
      float sm0 = 0.f, sm1 = 0.f;
#pragma unroll
      for (int j = 0; j < 4; ++j) { sm0 += lo16(xa[j]) + hi16(xa[j]); sm1 += lo16(xb[j]) + hi16(xb[j]); }
      if (lane >= 56) { sm0 = 0.f; sm1 = 0.f; }
      const float mean0 = wave_sum(sm0) * (1.f / D), mean1 = wave_sum(sm1) * (1.f / D);
      float v0 = 0.f, v1 = 0.f;
#pragma unroll
      for (int j = 0; j < 4; ++j) {
        float d = lo16(xa[j]) - mean0; v0 = fmaf(d, d, v0); d = hi16(xa[j]) - mean0; v0 = fmaf(d, d, v0);
        d = lo16(xb[j]) - mean1; v1 = fmaf(d, d, v1); d = hi16(xb[j]) - mean1; v1 = fmaf(d, d, v1);
      }
      if (lane >= 56) { v0 = 0.f; v1 = 0.f; }
      const float rs0 = ln_rsd(wave_sum(v0) * (1.f / D)), rs1 = ln_rsd(wave_sum(v1) * (1.f / D));
      const float mb0 = rbf(mean0), mb1 = rbf(mean1);
      const float n00 = rbf(rbf(lo16(xd0) - mb0) * rs0), n01 = rbf(rbf(hi16(xd0) - mb0) * rs0);
      const float n10 = rbf(rbf(lo16(xd1) - mb1) * rs1), n11 = rbf(rbf(hi16(xd1) - mb1) * rs1);
      {
        int off[2] = {WS_MOD + ((qk0 + qr) >> 1) * 16, WS_MOD + (224 + ((qk0 + qr) >> 1)) * 16}; v4u g[2];
        nap_until(tstart, G_MOD);
        gather<2, PSLP>(rs, off, g, T);
        STAMP(2);
        const float h00 = rbf(n00 * rbf(1.f + lo16(g[1].x))) + lo16(g[0].x), h10 = rbf(n10 * rbf(1.f + hi16(g[1].x))) + hi16(g[0].x);
        const float h01 = rbf(n01 * rbf(1.f + lo16(g[1].z))) + lo16(g[0].z), h11 = rbf(n11 * rbf(1.f + hi16(g[1].z))) + hi16(g[0].z);
        const bool live = 2 * lane < qnr;
        *(LAS unsigned*)(L + L_H1 + lane * 4) = live ? pack2(h00, h01) : 0u;
        *(LAS unsigned*)(L + L_H1 + 256 + lane * 4) = live ? pack2(h10, h11) : 0u;
      }
      {
        v4f d[4];
#pragma unroll
        for (int t = 0; t < 4; ++t) d[t] = v4f{0.f, 0.f, 0.f, 0.f};
#pragma unroll
        for (int s = 0; s < 4; ++s) {
          const v4u aa = *(const LAS v4u*)(L + L_H1 + (lane & 1) * 256 + s * 64 + g16 * 16);
#pragma unroll
          for (int t = 0; t < 4; ++t) d[t] = mfma(aa, bq[s * 4 + t], d[t]);
        }
        const float q0 = g16 == 0 ? d[0][0] : (g16 == 1 ? d[1][0] : (g16 == 2 ? d[2][0] : d[3][0]));
        const float q1 = g16 == 0 ? d[0][1] : (g16 == 1 ? d[1][1] : (g16 == 2 ? d[2][1] : d[3][1]));
        const bool odd = lane & 1;
        const float rcv = dpp<XOR1>(odd ? q0 : q1);
        publish(rs, WS_Q + ((((lane & 1) * 16 + h) * 4 + rq) * 128 + cbq * 32 + (lane >> 1)) * 16,
                __float_as_uint(odd ? rcv : q0), __float_as_uint(odd ? q1 : rcv), T);
      }
      STAMP(3);
      {
        int off[8]; v4u g[8];
#pragma unroll
        for (int k = 0; k < 8; ++k) off[k] = WS_Q + bh * 8192 + (k * 64 + lane) * 16;
        nap_until(now(), G_Q);
        gather<8, PSLP>(rs, off, g, T);
#pragma unroll
        for (int u = 0; u < 2; ++u) {
          const float qa = (__uint_as_float(g[u].x) + __uint_as_float(g[2 + u].x)) + (__uint_as_float(g[4 + u].x) + __uint_as_float(g[6 + u].x));
          const float qb = (__uint_as_float(g[u].z) + __uint_as_float(g[2 + u].z)) + (__uint_as_float(g[4 + u].z) + __uint_as_float(g[6 + u].z));
          *(LAS unsigned*)(L + L_Q + (u * 64 + lane) * 4) = pack2(qa, qb);
        }
        waitlds();
        if (lane == 0) flags[F_Q] = 1u;
      }
      STAMP(4);
      lds_wait(flags + F_SC, 8u);
      {
        int off[16]; v4u g[16];
#pragma unroll
        for (int k = 0; k < 16; ++k) off[k] = WS_SC + bh * 16384 + (((k ^ (kh * 8)) * 64) + lane) * 16;
        nap_until(now(), G_SC);
        gather<16, PSLP>(rs, off, g, T);
        STAMP(5);
        float m = -INFINITY;
#pragma unroll
        for (int k = 0; k < 16; ++k) m = fmaxf(fmaxf(fmaxf(lo16(g[k].x), hi16(g[k].x)), fmaxf(lo16(g[k].z), hi16(g[k].z))), m);
        m = wave_max(m);
        const float cs = 0.0625f * 1.4426950408889634f;
        const float mc = m * cs;
        float e[32]; float l = 0.f;
#pragma unroll
        for (int k = 0; k < 16; ++k) {
          const float e0_ = __builtin_amdgcn_exp2f(fmaf(lo16(g[k].x), cs, -mc)), e1_ = __builtin_amdgcn_exp2f(fmaf(hi16(g[k].x), cs, -mc));
          const float e2_ = __builtin_amdgcn_exp2f(fmaf(lo16(g[k].z), cs, -mc)), e3_ = __builtin_amdgcn_exp2f(fmaf(hi16(g[k].z), cs, -mc));
          l += (e0_ + e1_) + (e2_ + e3_);
          if (k < 8) { e[4 * k] = e0_; e[4 * k + 1] = e1_; e[4 * k + 2] = e2_; e[4 * k + 3] = e3_; }
        }
        l = wave_sum(l);
        const float rl = rcp_nr(l);
        const int t = lane & 15;
        const int half = t >> 3, j0 = (t >> 2) & 1, j1 = (t >> 1) & 1, j4 = t & 1;
#pragma unroll
        for (int k = 0; k < 8; ++k) {
          const int cq = k >> 1;
          const int wp = (k & 1) * 4 + (lane >> 4);
          const int wc = (j1 << 2) | (j0 << 1) | (wp >> 2), gg = ((wp & 3) << 1) | half, mm = (cq << 2) | (j4 << 1);
          *(LAS v2u*)(L + L_P + ((wc * 8 + gg) * 16 + mm) * 4) = v2u{pack2(e[4 * k] * rl, e[4 * k + 1] * rl), pack2(e[4 * k + 2] * rl, e[4 * k + 3] * rl)};
        }
        waitlds();
        if (lane == 0) flags[F_P] = 1u;
      }
      STAMP(6);
    } else if (a == 1) {
      lds_wait(flags + F_KI, 1u);
      const GAS unsigned char* src = (const GAS unsigned char*)p.wo + (size_t)(h * 256 + dq * 64) * (D * 2) + lane * 16;
#pragma unroll
      for (int i = 0; i < 56; ++i) {
        if (i >= WDEPTH) waitvm<WDEPTH - 1>();
        __builtin_amdgcn_global_load_lds((const GAS unsigned*)(src + i * 1024), (LAS unsigned*)(L + L_WO + i * 1024), 16, 0, POL_W);
      }
      waitvm<0>();
      if (lane == 0) flags[F_WO] = 1u;
      STAMP(2);
    } else if (a == 3) {
      if (is_red) {
        const unsigned xr = __builtin_amdgcn_raw_buffer_load_b32(rx, (rb * D + rchunk * 8 + (lane & 3) * 2) * 2, 0, 0);
        int off[2] = {WS_MOD + (D + rchunk * 4 + (lane & 3)) * 16, WS_MOD + (5 * D / 2 + rchunk * 4 + (lane & 3)) * 16}; v4u g[2];
        lds_wait(flags + F_KI, 1u);
        gather<2, 32>(rs, off, g, T);
        kp[0] = rb ? hi16(g[0].x) : lo16(g[0].x); kp[1] = rb ? hi16(g[0].z) : lo16(g[0].z);
        kp[2] = rb ? hi16(g[1].x) : lo16(g[1].x); kp[3] = rb ? hi16(g[1].z) : lo16(g[1].z);
        kp[4] = lo16(xr); kp[5] = hi16(xr);
        STAMP(2);
      }
    }
    if (is_ffn) {
      const int col2 = ((a & 1) * 56 + l56) * 2;
      int off[4] = {WS_MOD + (2 * D + col2) * 16, WS_MOD + (2 * D + col2 + 1) * 16, WS_MOD + (3 * D / 2 + col2) * 16, WS_MOD + (3 * D / 2 + col2 + 1) * 16};
      v4u g[4];
      lds_wait(flags + F_KI, 1u);
      gather<4, 32>(rs, off, g, T);
      const bool hb = a >> 1;
      kp[0] = rbf(1.f + (hb ? hi16(g[0].x) : lo16(g[0].x))); kp[1] = rbf(1.f + (hb ? hi16(g[0].z) : lo16(g[0].z)));
      kp[2] = rbf(1.f + (hb ? hi16(g[1].x) : lo16(g[1].x))); kp[3] = rbf(1.f + (hb ? hi16(g[1].z) : lo16(g[1].z)));
      kp[4] = hb ? hi16(g[2].x) : lo16(g[2].x); kp[5] = hb ? hi16(g[2].z) : lo16(g[2].z);
      kp[6] = hb ? hi16(g[3].x) : lo16(g[3].x); kp[7] = hb ? hi16(g[3].z) : lo16(g[3].z);
    }
    lds_wait(flags + F_WO, 1u);
#pragma unroll
    for (int s = 0; s < 2; ++s) {
      bw[s] = bfrag(L + L_WO + (32 * s + tr_row) * (D * 2) + t0 * 32 + tr_col, D * 2);
      bw[2 + s] = bfrag(L + L_WO + (32 * s + tr_row) * (D * 2) + t1 * 32 + tr_col, D * 2);
      bw[4 + s] = bfrag(L + L_WO + (32 * s + tr_row) * (D * 2) + t2 * 32 + tr_col, D * 2);
    }
    STAMP(7);
  }

  bar();
  const int n16 = lane & 15;
  const int tsel = g16 == 0 ? t0 : (g16 == 2 ? t1 : t2);
  {
    const v4u a0 = *(const LAS v4u*)(L + L_OA + n16 * 144 + g16 * 16), a1 = *(const LAS v4u*)(L + L_OA + n16 * 144 + 64 + g16 * 16);
    const v4f z = {0.f, 0.f, 0.f, 0.f};
    const v4f d0 = mfma(a1, bw[1], mfma(a0, bw[0], z)), d1 = mfma(a1, bw[3], mfma(a0, bw[2], z)), d2 = mfma(a1, bw[5], mfma(a0, bw[4], z));
    const float u1 = tr32((d0[0] + d0[1]) + (d0[2] + d0[3]), (d1[0] + d1[1]) + (d1[2] + d1[3]));
    const float u2 = sum32((d2[0] + d2[1]) + (d2[2] + d2[3]));
    const float v = tr16(u1, u2);
    const float vn = dpp<XOR1>(v);
    const int n = tsel * 16 + n16;
    if ((lane & 1) == 0 && ((g16 & 1) == 0 || (g16 == 1 && wave < 4)))
      publish(rs, WS_P1 + (((b * 56 + (n >> 3)) * 128 + h * 8 + c) * 4 + ((n >> 1) & 3)) * 16, __float_as_uint(v), __float_as_uint(vn), T);
    STAMP(8);
  }
  if (is_red) {
    if (wave == 3) {
      float x20, x21;
      {
        int off[8]; v4u g[8];
#pragma unroll
        for (int k = 0; k < 8; ++k) off[k] = WS_P1 + (rb * 56 + rchunk) * 8192 + (k * 64 + lane) * 16;
        gather<8, 1>(rs, off, g, T);
        STAMP(9);
        float s0 = 0.f, s1 = 0.f;
#pragma unroll
        for (int k = 0; k < 8; k += 2) { s0 += __uint_as_float(g[k].x) + __uint_as_float(g[k + 1].x); s1 += __uint_as_float(g[k].z) + __uint_as_float(g[k + 1].z); }
        s0 += dpp<ROR4>(s0); s1 += dpp<ROR4>(s1); s0 += dpp<ROR8>(s0); s1 += dpp<ROR8>(s1);
        s0 = sum16(s0); s1 = sum16(s1); s0 = sum32(s0); s1 = sum32(s1);
        x20 = rbf(kp[4] + rbf(kp[0] * rbf(s0))); x21 = rbf(kp[5] + rbf(kp[1] * rbf(s1)));
        const unsigned xp = pack2(x20, x21), xq = dppu<XOR1>(xp);
        float sm = x20 + x21; sm += dpp<XOR1>(sm); sm += dpp<XOR2>(sm);
        const float d0 = x20 - sm * 0.125f, d1 = x21 - sm * 0.125f;
        float m2 = fmaf(d0, d0, d1 * d1); m2 += dpp<XOR1>(m2); m2 += dpp<XOR2>(m2);
        const bool stl = lane == 1;
        if (lane < 3) publish(rs, stl ? WS_ST + (rb * 56 + rchunk) * 16 : WS_X2 + (rb * 112 + rchunk * 2 + (lane >> 1)) * 16,
                              stl ? __float_as_uint(sm) : xp, stl ? __float_as_uint(m2) : xq, T);
        STAMP(10);
      }
      {
        int off[4]; v4u g[4];
#pragma unroll
        for (int k = 0; k < 4; ++k) { int pc = k * 64 + lane; pc = pc < 224 ? pc : pc - 32; off[k] = WS_P2 + (rb * 56 + rchunk) * (NF * 64) + pc * 16; }
        gather<4, 1>(rs, off, g, T);
        float s0 = (__uint_as_float(g[0].x) + __uint_as_float(g[1].x)) + (__uint_as_float(g[2].x) + (lane < 32 ? __uint_as_float(g[3].x) : 0.f));
        float s1 = (__uint_as_float(g[0].z) + __uint_as_float(g[1].z)) + (__uint_as_float(g[2].z) + (lane < 32 ? __uint_as_float(g[3].z) : 0.f));
        s0 += dpp<ROR4>(s0); s1 += dpp<ROR4>(s1); s0 += dpp<ROR8>(s0); s1 += dpp<ROR8>(s1);
        s0 = sum16(s0); s1 = sum16(s1); s0 = sum32(s0); s1 = sum32(s1);
        const unsigned ov = pack2(x20 + rbf(kp[2] * rbf(s0)), x21 + rbf(kp[3] * rbf(s1)));
        if (lane < 4) ((GAS unsigned*)p.out)[(rb * D + rchunk * 8) / 2 + lane] = ov;
        if (bid == 13 * 8 && lane == 0) __builtin_amdgcn_raw_buffer_store_b32(T, rs, WS_EPOCH, 0, AUX_SC1);
        STAMP(11);
      }
    }
  } else if (is_ffn) {
    {
      const GAS unsigned char* gsrc = (const GAS unsigned char*)((lane & 4) ? p.wu : p.wg) + (size_t)(lane >> 3) * (F * 2) + fidx * 64 + (lane & 3) * 16;
      const GAS unsigned char* dsrc = (const GAS unsigned char*)p.wd + (size_t)fidx * 32 * (D * 2) + lane * 16;
#pragma unroll
      for (int i = 0; i < 7; ++i) {
        const int u = wave * 7 + i;
        const GAS unsigned char* src = u < 56 ? gsrc + (size_t)u * 8 * (F * 2) : dsrc + (u - 56) * 1024;
        __builtin_amdgcn_global_load_lds((const GAS unsigned*)src, (LAS unsigned*)(L + L_GU + u * 1024), 16, 0, POL_W);
      }
      waitvm<0>();
      if (wave < 4) STAMP(1);
      bar();
    }
    v4u bd[3];
    bd[0] = bfrag(L + L_DN + tr_row * (D * 2) + t0 * 32 + tr_col, D * 2);
    bd[1] = bfrag(L + L_DN + tr_row * (D * 2) + t1 * 32 + tr_col, D * 2);
    bd[2] = bfrag(L + L_DN + tr_row * (D * 2) + t2 * 32 + tr_col, D * 2);
    if (wave < 4) {
      v4u bg[14];
      {
        LAS unsigned char* gq = L + L_GU + tr_row * 128 + (lane & 2) * 32 + wave * 16 + (lane & 1) * 8;
#pragma unroll
        for (int s = 0; s < 14; ++s) bg[s] = bfrag(gq + s * 32 * 128, 128);
      }
      const int l56 = lane < 56 ? lane : 55, xbt = wave >> 1;
      {
        int off[2] = {WS_X2 + (wave * 56 + l56) * 16, WS_ST + (xbt * 56 + l56) * 16}; v4u g[2];
        gather<2, 1>(rs, off, g, T);
        STAMP(9);
        const float sm = lane < 56 ? __uint_as_float(g[1].x) : 0.f;
        const float mean = wave_sum(sm) * (1.f / D);
        const float dm = sm * 0.125f - mean;
        const float m2 = wave_sum(lane < 56 ? fmaf(8.f * dm, dm, __uint_as_float(g[1].z)) : 0.f);
        const float rsd = ln_rsd(m2 * (1.f / D)), mbf = rbf(mean);
        const float h0 = rbf(rbf(rbf(lo16(g[0].x) - mbf) * rsd) * kp[0]) + kp[4], h1 = rbf(rbf(rbf(hi16(g[0].x) - mbf) * rsd) * kp[1]) + kp[5];
        const float h2 = rbf(rbf(rbf(lo16(g[0].z) - mbf) * rsd) * kp[2]) + kp[6], h3 = rbf(rbf(rbf(hi16(g[0].z) - mbf) * rsd) * kp[3]) + kp[7];
        *(LAS v2u*)(L + L_H2 + (wave * 56 + l56) * 8) = v2u{pack2(h0, h1), pack2(h2, h3)};
      }
      bar();
      v4f da = {0.f, 0.f, 0.f, 0.f}, db = {0.f, 0.f, 0.f, 0.f};
#pragma unroll
      for (int s = 0; s < 14; s += 2) {
        const v4u a0 = *(const LAS v4u*)(L + L_H2 + (lane & 1) * (D * 2) + s * 64 + g16 * 16);
        const v4u a1 = *(const LAS v4u*)(L + L_H2 + (lane & 1) * (D * 2) + s * 64 + 64 + g16 * 16);
        da = mfma(a0, bg[s], da); db = mfma(a1, bg[s + 1], db);
      }
      const float g0 = rbf(da[0] + db[0]), g1 = rbf(da[1] + db[1]);
      const float u0 = dpp<ROR8>(g0), u1 = dpp<ROR8>(g1);
      const float k1 = -2.f * 0.7978845608028654f * 1.4426950408889634f;
      const float e0_ = __builtin_amdgcn_exp2f(k1 * fmaf(0.044715f * g0 * g0, g0, g0)), e1_ = __builtin_amdgcn_exp2f(k1 * fmaf(0.044715f * g1 * g1, g1, g1));
      const float ge0 = rbf(g0 * __builtin_amdgcn_rcpf(1.f + e0_)), ge1 = rbf(g1 * __builtin_amdgcn_rcpf(1.f + e1_));
      const unsigned av = pack2(ge0 * u0, ge1 * u1);
      if (lane < 8) {
        *(LAS u16*)(L + L_ACT + (wave * 8 + lane) * 2) = (u16)av;
        *(LAS u16*)(L + L_ACT + 64 + (wave * 8 + lane) * 2) = (u16)(av >> 16);
      }
      STAMP(10);
    } else {
      bar();
    }
    bar();
    {
      const v4u aa = *(const LAS v4u*)(L + L_ACT + (lane & 1) * 64 + g16 * 16);
      const v4f z = {0.f, 0.f, 0.f, 0.f};
      const v4f d0 = mfma(aa, bd[0], z), d1 = mfma(aa, bd[1], z), d2 = mfma(aa, bd[2], z);
      const float v0 = g16 == 0 ? d0[0] : (g16 == 2 ? d1[0] : d2[0]), v1 = g16 == 0 ? d0[1] : (g16 == 2 ? d1[1] : d2[1]);
      const bool odd = lane & 1;
      const float rcv = dpp<XOR1>(odd ? v0 : v1);
      const int n = tsel * 16 + n16;
      if ((g16 & 1) == 0 || (g16 == 1 && wave < 4))
        publish(rs, WS_P2 + ((((lane & 1) * 56 + (n >> 3)) * NF + fidx) * 4 + ((n >> 1) & 3)) * 16,
                __float_as_uint(odd ? rcv : v0), __float_as_uint(odd ? v1 : rcv), T);
      STAMP(11);
    }
  }
  STAMP_FLUSH();
}
}

extern "C" void kernel_launch(void* const* d_in, const int* in_sizes, int n_in, void* d_out, int out_size, void* d_ws,
                              size_t ws_size, hipStream_t stream) {
  Params p;
  p.x = (const u16*)d_in[0]; p.kc = (const u16*)d_in[1]; p.vc = (const u16*)d_in[2];
  p.wq = (const u16*)d_in[3]; p.wo = (const u16*)d_in[4];
  p.wg = (const u16*)d_in[6]; p.wu = (const u16*)d_in[7]; p.wd = (const u16*)d_in[8];
  p.c = (const u16*)d_in[9]; p.wmod = (const u16*)d_in[10];
  p.out = (u16*)d_out; p.ws = (unsigned char*)d_ws;
  (void)hipFuncSetAttribute((const void*)fused_block, hipFuncAttributeMaxDynamicSharedMemorySize, L_END);
  hipLaunchKernelGGL(fused_block, dim3(256), dim3(NTHR), L_END, stream, p);
}
```

```cpp
#include <hip/hip_runtime.h>
#include <stdint.h>

typedef unsigned short u16;
typedef unsigned v4u __attribute__((ext_vector_type(4)));
typedef unsigned v2u __attribute__((ext_vector_type(2)));
typedef float v4f __attribute__((ext_vector_type(4)));
typedef float v2f __attribute__((ext_vector_type(2)));
typedef short v8s __attribute__((ext_vector_type(8)));
typedef short v4s __attribute__((ext_vector_type(4)));
typedef __bf16 bf16x2 __attribute__((ext_vector_type(2)));
#define GAS __attribute__((address_space(1)))
#define LAS __attribute__((address_space(3)))

#define KDEPTH 3
#define VDEPTH 3
#define WDEPTH 8
#define PSTART 26
#define G_MOD 450
#define G_Q 150
#define G_SC 100
#define TBIG 16
#define TTHR 64
#define G_MP 60
#define PRETOUCH 1
#define VEARLY 8
#define HOLDL 0
#define EFROM 32
#define EDEPTH 6
#define FSTART 31
#define DUALP 0
#define L2WARM 1
#define PTHR 38
#define HOLDK 0
#define PSLP 24
#define POL_K 2
#define POL_V 2
#define POL_W 2
#define DBG_STAMP 0

namespace {
constexpr int D = 448, NH = 16, SK = 4096, DH = 256, F = 1792;
constexpr int NTHR = 768;
constexpr int NF = 56;
constexpr int AUX_NT = 2, AUX_SC1 = 16;
constexpr unsigned SPIN_CAP = 1u << 16;

constexpr int WS_EPOCH = 0;
constexpr int NREP = 8;
constexpr int MOD_REP = 21504, XS_REP = 5376;
constexpr int WS_MOD = 256;
constexpr int WS_X2 = WS_MOD + NREP * MOD_REP;
constexpr int WS_ST = WS_X2 + 3584;
constexpr int WS_Q = 262144;
static_assert(WS_X2 + NREP * XS_REP <= WS_Q, "ws map");
constexpr int WS_SC = WS_Q + 32 * 8192;
constexpr int WS_P1 = WS_SC + 32 * 16384;
constexpr int WS_P2 = WS_P1 + 2 * 56 * 8192;
constexpr int WS_MP = WS_P2 + 2 * 56 * NF * 64;
constexpr int WS_END = WS_MP + 42 * 3 * 1024;
constexpr int WS_STAMP = 4 << 20;
static_assert(WS_END <= WS_STAMP, "ws map");

constexpr int L_WO = 0;
constexpr int L_GU = 57344;
constexpr int L_DN = L_GU + 57344;
constexpr int L_Q = L_DN + 28672;
constexpr int L_H1 = L_Q + 512;
constexpr int L_P = L_H1 + 512;
constexpr int L_OA = L_P + 4096;
constexpr int L_H2 = L_OA + 2304;
constexpr int L_WM = L_GU;
constexpr int L_VE = L_GU;
constexpr int L_WQ = L_DN;
constexpr int L_ACT = L_H2 + 1792;
constexpr int L_FLAG = L_ACT + 128;
constexpr int L_LAT = L_FLAG + 64;
constexpr int L_END = L_LAT + 32;
static_assert(L_END <= 160 * 1024 && L_END > 80 * 1024, "lds");
enum { F_Q = 0, F_P = 1, F_WO = 2, F_SC = 3, F_MOD = 4, F_KI = 5, F_TV = 6, F_U = 7, F_T = 8, F_PUB = 9, F_O = 10 };

struct Params {
  const u16 *x, *kc, *vc, *wq, *wo, *wg, *wu, *wd, *c, *wmod;
  u16* out;
  unsigned char* ws;
};

__device__ __forceinline__ float lo16(unsigned w) { return __uint_as_float(w << 16); }
__device__ __forceinline__ float hi16(unsigned w) { return __uint_as_float(w & 0xffff0000u); }
__device__ __forceinline__ unsigned pack2(float a, float b) {
  bf16x2 v = {(__bf16)a, (__bf16)b};
  return __builtin_bit_cast(unsigned, v);
}
__device__ __forceinline__ float rbf(float f) { return lo16(pack2(f, f)); }
__device__ __forceinline__ float dot2(unsigned a, unsigned b, float c) {
  return __builtin_amdgcn_fdot2_f32_bf16(__builtin_bit_cast(bf16x2, a), __builtin_bit_cast(bf16x2, b), c, false);
}
__device__ __forceinline__ float rcp_nr(float x) { const float r = __builtin_amdgcn_rcpf(x); return fmaf(fmaf(-x, r, 1.f), r, r); }
template <int N> __device__ __forceinline__ void waitvm() {
  asm volatile("" ::: "memory");
  __builtin_amdgcn_s_waitcnt((N & 15) | ((N >> 4) << 14) | 0x0F70);
  asm volatile("" ::: "memory");
}
__device__ __forceinline__ void waitlds() { asm volatile("s_waitcnt lgkmcnt(0)" ::: "memory"); }
__device__ __forceinline__ void bar() {
  __builtin_amdgcn_sched_barrier(0);
  asm volatile("s_waitcnt lgkmcnt(0)" ::: "memory");
  __builtin_amdgcn_s_barrier();
  asm volatile("" ::: "memory");
  __builtin_amdgcn_sched_barrier(0);
}
template <int CTRL> __device__ __forceinline__ float dpp(float x) {
  return __builtin_bit_cast(float, __builtin_amdgcn_mov_dpp(__builtin_bit_cast(int, x), CTRL, 0xf, 0xf, true));
}
template <int CTRL> __device__ __forceinline__ unsigned dppu(unsigned x) {
  return (unsigned)__builtin_amdgcn_mov_dpp((int)x, CTRL, 0xf, 0xf, true);
}
constexpr int XOR1 = 0xB1, XOR2 = 0x4E, XOR7 = 0x141, ROR8 = 0x128, ROR4 = 0x124;
__device__ __forceinline__ float tr32(float a, float b) {
  auto r = __builtin_amdgcn_permlane32_swap(__float_as_uint(a), __float_as_uint(b), false, false);
  return __uint_as_float(r[0]) + __uint_as_float(r[1]);
}
__device__ __forceinline__ float tr16(float a, float b) {
  auto r = __builtin_amdgcn_permlane16_swap(__float_as_uint(a), __float_as_uint(b), false, false);
  return __uint_as_float(r[0]) + __uint_as_float(r[1]);
}
template <int CTRL> __device__ __forceinline__ float trd(float a, float b, bool bit) {
  const float t1 = a + dpp<CTRL>(a), t2 = b + dpp<CTRL>(b);
  return bit ? t2 : t1;
}
__device__ __forceinline__ float sum32(float a) { return tr32(a, a); }
__device__ __forceinline__ float sum16(float a) { return tr16(a, a); }
__device__ __forceinline__ float wave_sum(float v) {
  v += dpp<XOR1>(v); v += dpp<XOR2>(v); v += dpp<XOR7>(v); v += dpp<ROR8>(v);
  v = sum16(v); v = sum32(v);
  return v;
}
__device__ __forceinline__ float wave_max(float v) {
  v = fmaxf(v, dpp<XOR1>(v)); v = fmaxf(v, dpp<XOR2>(v)); v = fmaxf(v, dpp<XOR7>(v)); v = fmaxf(v, dpp<ROR8>(v));
  auto r = __builtin_amdgcn_permlane16_swap(__float_as_uint(v), __float_as_uint(v), false, false);
  v = fmaxf(__uint_as_float(r[0]), __uint_as_float(r[1]));
  auto t = __builtin_amdgcn_permlane32_swap(__float_as_uint(v), __float_as_uint(v), false, false);
  return fmaxf(__uint_as_float(t[0]), __uint_as_float(t[1]));
}
__device__ __forceinline__ v4f mfma(v4u a, v4u b, v4f c) {
  return __builtin_amdgcn_mfma_f32_16x16x32_bf16(__builtin_bit_cast(v8s, a), __builtin_bit_cast(v8s, b), c, 0, 0, 0);
}
__device__ __forceinline__ v4u bfrag(LAS unsigned char* q, int stride) {
  const v2u a = __builtin_bit_cast(v2u, __builtin_amdgcn_ds_read_tr16_b64_v4i16((LAS v4s*)q));
  const v2u b = __builtin_bit_cast(v2u, __builtin_amdgcn_ds_read_tr16_b64_v4i16((LAS v4s*)(q + 4 * stride)));
  return v4u{a.x, a.y, b.x, b.y};
}

__device__ __forceinline__ void glds16(const GAS unsigned char* gsrc, unsigned lds_dst) {
  unsigned keep;
  asm volatile("s_mov_b32 %0, m0\n\ts_mov_b32 m0, %2\n\ts_nop 0\n\tglobal_load_lds_dwordx4 %1, off\n\ts_mov_b32 m0, %0"
               : "=&s"(keep) : "v"(gsrc), "s"(lds_dst) : "memory");
}
__device__ __forceinline__ void glds16_nt(const GAS unsigned char* gsrc, unsigned lds_dst) {
  unsigned keep;
  asm volatile("s_mov_b32 %0, m0\n\ts_mov_b32 m0, %2\n\ts_nop 0\n\tglobal_load_lds_dwordx4 %1, off nt\n\ts_mov_b32 m0, %0"
               : "=&s"(keep) : "v"(gsrc), "s"(lds_dst) : "memory");
}
typedef __amdgpu_buffer_rsrc_t rsrc_t;
__device__ __forceinline__ v4u ldstream(rsrc_t r, int voff, int soff, bool keep) {
  if (keep) return __builtin_amdgcn_raw_buffer_load_b128(r, voff, soff, 0);
  return __builtin_amdgcn_raw_buffer_load_b128(r, voff, soff, AUX_NT);
}
__device__ __forceinline__ rsrc_t mkrsrc(const void* p, int bytes) { return __builtin_amdgcn_make_buffer_rsrc((void*)p, 0, bytes, 0x00020000); }
__device__ __forceinline__ void publish(rsrc_t rs, int off, unsigned a, unsigned b, unsigned T) {
  v4u v = {a, T, b, T};
  __builtin_amdgcn_raw_buffer_store_b128(v, rs, off, 0, AUX_SC1);
}
template <int N, int SLP> __device__ __forceinline__ void gather(rsrc_t rs, const int (&off)[N], v4u (&v)[N], unsigned T) {
#pragma unroll
  for (int k = 0; k < N; ++k) v[k] = __builtin_amdgcn_raw_buffer_load_b128(rs, off[k], 0, AUX_SC1);
  for (unsigned spins = 0; spins < SPIN_CAP; ++spins) {
    bool ok = true;
#pragma unroll
    for (int k = 0; k < N; ++k) ok &= (v[k].y == T) & (v[k].w == T);
    const int miss = __builtin_popcountll(__builtin_amdgcn_ballot_w64(!ok));
    if (miss == 0) break;
    if (SLP < TBIG && miss > TTHR) __builtin_amdgcn_s_sleep(TBIG); else __builtin_amdgcn_s_sleep(SLP);
    asm volatile("" ::: "memory");
#pragma unroll
    for (int k = 0; k < N; ++k)
      if ((v[k].y != T) | (v[k].w != T)) v[k] = __builtin_amdgcn_raw_buffer_load_b128(rs, off[k], 0, AUX_SC1);
  }
}
__device__ __forceinline__ void lds_wait(volatile LAS unsigned* f, unsigned want) {
  for (unsigned spins = 0; spins < (1u << 22); ++spins) {
    if (__builtin_amdgcn_readfirstlane(*f) >= want) break;
    __builtin_amdgcn_s_sleep(1);
  }
  asm volatile("" ::: "memory");
}
__device__ __forceinline__ void lds_spin(volatile LAS unsigned* f, unsigned want) {
  for (unsigned spins = 0; spins < (1u << 24); ++spins)
    if (__builtin_amdgcn_readfirstlane(*f) >= want) break;
  asm volatile("" ::: "memory");
}
__device__ __forceinline__ unsigned now() { return (unsigned)__builtin_amdgcn_s_memrealtime(); }
__device__ __forceinline__ void nap_until(unsigned t0, unsigned ticks) {
  for (int i = 0; i < 4096 && now() - t0 < ticks; ++i) __builtin_amdgcn_s_sleep(4);
}
__device__ __forceinline__ void lds_inc(volatile LAS unsigned* f) {
  __hip_atomic_fetch_add((LAS unsigned*)f, 1u, __ATOMIC_RELAXED, __HIP_MEMORY_SCOPE_WORKGROUP);
}
constexpr int pairs_done(int i) {
  if (i < PSTART) return 0;
  int a = (i - VDEPTH + 1) / 2, b = (i - PSTART + 1) * 3;
  if (i >= EFROM) { const int e = (i - EDEPTH + 1) / 2, n = (EFROM - VDEPTH) / 2; a = e > n ? e : n; }
  return a < b ? a : b;
}
__device__ __forceinline__ float ln_rsd(float var) { return rcp_nr(rbf(__builtin_amdgcn_sqrtf(rbf(rbf(var) + 1e-5f)))); }

#define STAMP(i)
#define STAMP_FLUSH()

__global__ void __launch_bounds__(NTHR, 3) fused_block(Params p) {
  extern __shared__ __attribute__((aligned(16))) unsigned char smem[];
  LAS unsigned char* L = (LAS unsigned char*)smem;
  volatile LAS unsigned* flags = (volatile LAS unsigned*)(L + L_FLAG);
  const int bid = blockIdx.x, tid = threadIdx.x, lane = tid & 63;
  const int wave = __builtin_amdgcn_readfirstlane(tid >> 6);
  const int xcd = bid & 7, li = bid >> 3;
#define CROT 0
  const int cu = xcd * 32 + ((li + CROT) & 31);
  const int h = cu >> 4, b = (cu >> 3) & 1, c = cu & 7, kh = c >> 2, dq = c & 3, bh = b * 16 + h;
  const bool q3 = (li & 3) == 3;
  const int jn = (li >> 2) * 3 + (li & 3);
  const int mu = q3 ? 136 + (li >> 2) * 8 + xcd : jn * 8 + xcd;
  const int mb = mu >> 2, mpart = mu & 3;
  const bool is_mod = q3 ? li < 16 : jn < 17, is_ffn = !q3 && jn >= 17, is_red = !q3 && jn < 14;
  const int fidx = xcd * 7 + jn - 17;
  const int rb = jn & 1, rchunk = xcd * 7 + (jn >> 1);
  asm volatile("" ::"s"(p.x), "s"(p.kc), "s"(p.vc), "s"(p.wq), "s"(p.wo), "s"(p.c), "s"(p.wmod), "s"(p.ws));
  const rsrc_t rs = mkrsrc(p.ws, WS_END);
  STAMP(0);
  const unsigned tstart = now();
  unsigned e0 = 0u;
  if (wave == 0) e0 = __builtin_amdgcn_raw_buffer_load_b32(rs, WS_EPOCH, 0, AUX_SC1);
  if (tid < 16) flags[tid] = 0u;
  unsigned T;
  v4u bw[8];
  float kp[8];
#pragma unroll
  for (int j = 0; j < 8; ++j) kp[j] = 0.f;
  const int t0 = wave < 4 ? 16 + wave : wave - 4, t1 = wave < 4 ? 20 + wave : wave + 4, t2 = wave < 4 ? 24 + wave : 0;
  const int w0 = wave < 4 ? 24 : wave - 4, wst = wave < 4 ? 1 : 8;
  const int tr_row = 8 * (lane >> 4) + ((lane >> 2) & 3), tr_col = (lane & 3) * 8, g16 = lane >> 4;

  if (wave >= 4) {
    const int sw = wave - 4;
    const rsrc_t rk = mkrsrc(p.kc, 64 << 20), rv = mkrsrc(p.vc, 64 << 20);
    const int koff = (bh * SK + c * 512) * 512 + sw * 1024 + lane * 16;
    const int voff = (bh * SK + kh * 2048 + sw * 8 + (lane >> 3)) * 512 + dq * 128 + (lane & 7) * 16;
    v4u kb[32], vb[32];
    constexpr bool keepk = false, keepv = false;
    if (bid < HOLDK) { bar(); lds_wait(flags + F_MOD, (mpart == 0 && HOLDL) ? 2u : 1u); }
#pragma unroll
    for (int i = 0; i < 32; ++i) {
      if (i == KDEPTH && bid >= HOLDK) bar();
      if (i >= KDEPTH) waitvm<KDEPTH - 1>();
      kb[i] = ldstream(rk, koff, i * 8192, keepk);
      __builtin_amdgcn_sched_barrier(0);
    }
    if (sw == 0 && lane == 0) flags[F_KI] = 1u;
    STAMP(1);
    lds_wait(flags + F_U, 2u);
    const GAS unsigned char* vsrc = (const GAS unsigned char*)p.vc + voff;
    const unsigned vdst = (unsigned)(size_t)(L + L_VE) + sw * 1024;
    int ev = 0;
    while (ev < VEARLY && __builtin_amdgcn_readfirstlane(flags[F_Q]) == 0u) {
      waitvm<VDEPTH - 1>();
      glds16_nt(vsrc + ev * 32768, vdst + ev * 8192);
      ++ev;
    }
    lds_wait(flags + F_Q, 1u);
    STAMP(2);
    const v4u qv = *(const LAS v4u*)(L + L_Q + (lane & 31) * 16);
    float s2[16];
#pragma unroll
    for (int m = 0; m < 16; ++m) {
      float a = 0.f, bb = 0.f;
#pragma unroll
      for (int j = 0; j < 4; ++j) { a = dot2(kb[2 * m][j], qv[j], a); bb = dot2(kb[2 * m + 1][j], qv[j], bb); }
      s2[m] = tr16(a, bb);
      if ((m & 3) == 3) {
        __builtin_amdgcn_sched_barrier(0);
        if (ev < VEARLY) { waitvm<VDEPTH - 1>(); glds16_nt(vsrc + ev * 32768, vdst + ev * 8192); ++ev; }
        waitvm<VDEPTH - 1>();
        vb[VEARLY + (m >> 2)] = ldstream(rv, voff, (VEARLY + (m >> 2)) * 32768, keepv);
        __builtin_amdgcn_sched_barrier(0);
      }
    }
    float s4[8], s8[4], s16[2];
#pragma unroll
    for (int u = 0; u < 8; ++u) s4[u] = trd<ROR8>(s2[2 * u], s2[2 * u + 1], lane & 8);
#pragma unroll
    for (int u = 0; u < 4; ++u) s8[u] = trd<XOR7>(s4[u], s4[u + 4], lane & 4);
#pragma unroll
    for (int u = 0; u < 2; ++u) s16[u] = trd<XOR2>(s8[u], s8[u + 2], lane & 2);
    const float sc = trd<XOR1>(s16[0], s16[1], lane & 1);
    T = __builtin_amdgcn_readfirstlane(flags[F_T]);
    {
      const unsigned pr = pack2(sc, dpp<XOR1>(sc));
      const unsigned pr2 = dppu<XOR2>(pr);
      if ((lane & 3) == 0) publish(rs, WS_SC + bh * 16384 + ((c * 8 + sw) * 16 + (lane >> 2)) * 16, pr, pr2, T);
      if (lane == 0) lds_inc(flags + F_SC);
    }
    STAMP(3);
    while (ev < VEARLY) { waitvm<VDEPTH - 1>(); glds16_nt(vsrc + ev * 32768, vdst + ev * 8192); ++ev; }
    float acc[8];
#pragma unroll
    for (int j = 0; j < 8; ++j) acc[j] = 0.f;
    v4u pp[4];
#pragma unroll
    for (int i = 4 + VEARLY; i <= 32; ++i) {
      if (i < 32) {
        __builtin_amdgcn_sched_barrier(0);
        if (i >= EFROM) waitvm<EDEPTH - 1>(); else waitvm<VDEPTH - 1>();
        vb[i] = ldstream(rv, voff, i * 32768, keepv);
        __builtin_amdgcn_sched_barrier(0);
      }
      if (i == PSTART) {
        STAMP(4);
        lds_wait(flags + F_P, 1u);
        STAMP(5);
#pragma unroll
        for (int u = 0; u < 4; ++u) pp[u] = *(const LAS v4u*)(L + L_P + (sw * 8 + (lane >> 3)) * 64 + u * 16);
      }
      if (i == 31) {
        lds_wait(flags + F_WO, 1u);
#pragma unroll
        for (int s = 0; s < 2; ++s) {
          bw[s] = bfrag(L + L_WO + (32 * s + tr_row) * (D * 2) + w0 * 32 + tr_col, D * 2);
          bw[2 + s] = bfrag(L + L_WO + (32 * s + tr_row) * (D * 2) + (w0 + 8) * 32 + tr_col, D * 2);
          bw[4 + s] = bfrag(L + L_WO + (32 * s + tr_row) * (D * 2) + (w0 + 16) * 32 + tr_col, D * 2);
          bw[6 + s] = v4u{0u, 0u, 0u, 0u};
        }
      }
      const int m0 = pairs_done(i - 1), m1 = i < 32 ? pairs_done(i) : 16;
#pragma unroll
      for (int m = m0; m < m1; ++m) {
        const unsigned pw = pp[m >> 2][m & 3];
        const v4u va = 2 * m < VEARLY ? *(const LAS v4u*)(L + L_VE + (2 * m * 8 + sw) * 1024 + lane * 16) : vb[2 * m];
        const v4u vc2 = 2 * m + 1 < VEARLY ? *(const LAS v4u*)(L + L_VE + ((2 * m + 1) * 8 + sw) * 1024 + lane * 16) : vb[2 * m + 1];
#pragma unroll
        for (int j = 0; j < 4; ++j) {
          const unsigned a = va[j], bb = vc2[j];
          acc[2 * j] = dot2(__builtin_amdgcn_perm(bb, a, 0x05040100u), pw, acc[2 * j]);
          acc[2 * j + 1] = dot2(__builtin_amdgcn_perm(bb, a, 0x07060302u), pw, acc[2 * j + 1]);
        }
      }
      if (m1 > m0) {
#pragma unroll
        for (int j = 0; j < 8; ++j) asm volatile("" : "+v"(acc[j]));
      }
    }
    STAMP(6);
    float a4[4], a2[2];
#pragma unroll
    for (int u = 0; u < 4; ++u) a4[u] = tr32(acc[u], acc[u + 4]);
#pragma unroll
    for (int u = 0; u < 2; ++u) a2[u] = tr16(a4[u], a4[u + 2]);
    const float o1 = trd<ROR8>(a2[0], a2[1], lane & 8);
    const int dim = (lane & 7) * 8 + ((lane >> 5) & 1) * 4 + ((lane >> 4) & 1) * 2 + ((lane >> 3) & 1);
    const float oh = rbf(o1);
    const unsigned hl = pack2(oh, o1 - oh);
    *(LAS u16*)(L + L_OA + sw * 144 + dim * 2) = (u16)hl;
    *(LAS u16*)(L + L_OA + (8 + sw) * 144 + dim * 2) = (u16)(hl >> 16);
    waitlds();
    if (lane == 0) lds_inc(flags + F_O);
  } else {
    const int a = wave;
    const rsrc_t rx = mkrsrc(p.x, 2 * D * 2);
    const int l56 = lane < 56 ? lane : 55, wsmod = WS_MOD + xcd * MOD_REP;
    v4u xa, xb; unsigned cv0, cv1, xd0, xd1;
    const int rq = (cu & 15) >> 2, cbq = cu & 3;
    const int qk0 = rq < 2 ? rq * 128 : 64 + rq * 96, qnr = rq < 2 ? 128 : 96;
    const int qr = 2 * lane < qnr ? 2 * lane : qnr - 2;
    const int mk0 = mpart < 2 ? mpart * 128 : 64 + mpart * 96, mnr = mpart < 2 ? 128 : 96;
    if (is_mod && a == 2) {
      const rsrc_t rc = mkrsrc(p.c, 2 * D * 2);
      const int mr = 2 * lane < mnr ? 2 * lane : mnr - 2;
      cv0 = __builtin_amdgcn_raw_buffer_load_b32(rc, (mk0 + mr) * 2, 0, 0);
      cv1 = __builtin_amdgcn_raw_buffer_load_b32(rc, (D + mk0 + mr) * 2, 0, 0);
      const GAS unsigned char* src = (const GAS unsigned char*)p.wmod + (size_t)(mk0 + (lane >> 3)) * (6 * D * 2) + mb * 128 + (lane & 7) * 16;
#pragma unroll
      for (int i = 0; i < 16; ++i)
        if (i < 12 || mpart < 2)
          __builtin_amdgcn_global_load_lds((const GAS unsigned*)(src + (size_t)i * 8 * (6 * D * 2)), (LAS unsigned*)(L + L_WM + i * 1024), 16, 0, POL_W);
    }
    if (a == 0) {
      xa = __builtin_amdgcn_raw_buffer_load_b128(rx, l56 * 16, 0, 0);
      xb = __builtin_amdgcn_raw_buffer_load_b128(rx, D * 2 + l56 * 16, 0, 0);
      xd0 = __builtin_amdgcn_raw_buffer_load_b32(rx, (qk0 + qr) * 2, 0, 0);
      xd1 = __builtin_amdgcn_raw_buffer_load_b32(rx, (D + qk0 + qr) * 2, 0, 0);
      const GAS unsigned char* src = (const GAS unsigned char*)p.wq + (size_t)(qk0 + (lane >> 3)) * (NH * DH * 2) + (h * 256 + cbq * 64) * 2 + (lane & 7) * 16;
#pragma unroll
      for (int i = 0; i < 16; ++i)
        if (i < 12 || rq < 2)
          __builtin_amdgcn_global_load_lds((const GAS unsigned*)(src + (size_t)i * 8 * (NH * DH * 2)), (LAS unsigned*)(L + L_WQ + i * 1024), 16, 0, POL_W);
    }
    bar();
    if (a == 0) {
      T = __builtin_amdgcn_readfirstlane(e0) + 1u;
      if (lane == 0) { flags[F_T] = T; waitlds(); flags[F_TV] = 1u; }
    } else {
      lds_wait(flags + F_TV, 1u);
      T = __builtin_amdgcn_readfirstlane(flags[F_T]);
    }
    if (!is_mod && a == 2 && lane == 0) lds_inc(flags + F_U);
    if (is_mod && a == 2) {
      {
        const float c00 = lo16(cv0), c01 = hi16(cv0), c10 = lo16(cv1), c11 = hi16(cv1);
        const float s00 = c00 / (1.f + __expf(-c00)), s01 = c01 / (1.f + __expf(-c01));
        const float s10 = c10 / (1.f + __expf(-c10)), s11 = c11 / (1.f + __expf(-c11));
        const bool live = 2 * lane < mnr;
        *(LAS unsigned*)(L + L_H2 + lane * 4) = live ? pack2(s00, s01) : 0u;
        *(LAS unsigned*)(L + L_H2 + 256 + lane * 4) = live ? pack2(s10, s11) : 0u;
      }
      waitvm<0>();
      if (lane == 0) flags[F_MOD] = 1u;
      v4f d[4];
#pragma unroll
      for (int t = 0; t < 4; ++t) d[t] = v4f{0.f, 0.f, 0.f, 0.f};
      LAS unsigned char* mq = L + L_WM + tr_row * 128 + tr_col;
#pragma unroll
      for (int s = 0; s < 4; ++s) {
        const v4u aa = *(const LAS v4u*)(L + L_H2 + (lane & 1) * 256 + s * 64 + g16 * 16);
#pragma unroll
        for (int t = 0; t < 4; ++t) d[t] = mfma(aa, bfrag(mq + (s < 3 || mpart < 2 ? s : 0) * 4096 + t * 32, 128), d[t]);
      }
      waitlds();
      if (lane == 0) lds_inc(flags + F_U);
      float m0 = g16 == 0 ? d[0][0] : (g16 == 1 ? d[1][0] : (g16 == 2 ? d[2][0] : d[3][0]));
      float m1 = g16 == 0 ? d[0][1] : (g16 == 1 ? d[1][1] : (g16 == 2 ? d[2][1] : d[3][1]));
      if (mpart != 0) {
        publish(rs, WS_MP + ((mb * 3 + mpart - 1) * 64 + lane) * 16, __float_as_uint(m0), __float_as_uint(m1), T);
      } else {
        int off[3]; v4u g[3];
#pragma unroll
        for (int k = 0; k < 3; ++k) off[k] = WS_MP + ((mb * 3 + k) * 64 + lane) * 16;
        nap_until(now(), G_MP);
        gather<3, 8>(rs, off, g, T);
        m0 = (m0 + __uint_as_float(g[0].x)) + (__uint_as_float(g[1].x) + __uint_as_float(g[2].x));
        m1 = (m1 + __uint_as_float(g[0].z)) + (__uint_as_float(g[1].z) + __uint_as_float(g[2].z));
        const unsigned gg = pack2(m0, m1), gn = dppu<XOR1>(gg);
        const unsigned lo_ = (lane & 1) ? gn : gg, hi_ = (lane & 1) ? gg : gn;
#pragma unroll
        for (int k = 0; k < NREP / 2; ++k) publish(rs, WS_MOD + (2 * k + (lane & 1)) * MOD_REP + (mb * 32 + (lane >> 1)) * 16, lo_, hi_, T);
        if (lane == 0) flags[F_MOD] = 2u;
      }
      STAMP(1);
    }
    if (a == 0) {
      waitvm<0>();
      v4u bq[16];
      {
        LAS unsigned char* qq = L + L_WQ + tr_row * 128 + tr_col;
#pragma unroll
        for (int s = 0; s < 4; ++s)
#pragma unroll
          for (int t = 0; t < 4; ++t) bq[s * 4 + t] = bfrag(qq + (s < 3 || rq < 2 ? s : 0) * 4096 + t * 32, 128);
      }
      waitlds();
      if (lane == 0) lds_inc(flags + F_U);
      float sm0 = 0.f, sm1 = 0.f;
#pragma unroll
      for (int j = 0; j < 4; ++j) { sm0 += lo16(xa[j]) + hi16(xa[j]); sm1 += lo16(xb[j]) + hi16(xb[j]); }
      if (lane >= 56) { sm0 = 0.f; sm1 = 0.f; }
      const float mean0 = wave_sum(sm0) * (1.f / D), mean1 = wave_sum(sm1) * (1.f / D);
      float v0 = 0.f, v1 = 0.f;
#pragma unroll
      for (int j = 0; j < 4; ++j) {
        float d = lo16(xa[j]) - mean0; v0 = fmaf(d, d, v0); d = hi16(xa[j]) - mean0; v0 = fmaf(d, d, v0);
        d = lo16(xb[j]) - mean1; v1 = fmaf(d, d, v1); d = hi16(xb[j]) - mean1; v1 = fmaf(d, d, v1);
      }
      if (lane >= 56) { v0 = 0.f; v1 = 0.f; }
      const float rs0 = ln_rsd(wave_sum(v0) * (1.f / D)), rs1 = ln_rsd(wave_sum(v1) * (1.f / D));
      const float mb0 = rbf(mean0), mb1 = rbf(mean1);
      const float n00 = rbf(rbf(lo16(xd0) - mb0) * rs0), n01 = rbf(rbf(hi16(xd0) - mb0) * rs0);
      const float n10 = rbf(rbf(lo16(xd1) - mb1) * rs1), n11 = rbf(rbf(hi16(xd1) - mb1) * rs1);
      {
        int off[2] = {wsmod + ((qk0 + qr) >> 1) * 16, wsmod + (224 + ((qk0 + qr) >> 1)) * 16}; v4u g[2];
        nap_until(tstart, G_MOD);
        gather<2, PSLP>(rs, off, g, T);
        STAMP(2);
        const float h00 = rbf(n00 * rbf(1.f + lo16(g[1].x))) + lo16(g[0].x), h10 = rbf(n10 * rbf(1.f + hi16(g[1].x))) + hi16(g[0].x);
        const float h01 = rbf(n01 * rbf(1.f + lo16(g[1].z))) + lo16(g[0].z), h11 = rbf(n11 * rbf(1.f + hi16(g[1].z))) + hi16(g[0].z);
        const bool live = 2 * lane < qnr;
        *(LAS unsigned*)(L + L_H1 + lane * 4) = live ? pack2(h00, h01) : 0u;
        *(LAS unsigned*)(L + L_H1 + 256 + lane * 4) = live ? pack2(h10, h11) : 0u;
      }
      {
        v4f d[4];
#pragma unroll
        for (int t = 0; t < 4; ++t) d[t] = v4f{0.f, 0.f, 0.f, 0.f};
#pragma unroll
        for (int s = 0; s < 4; ++s) {
          const v4u aa = *(const LAS v4u*)(L + L_H1 + (lane & 1) * 256 + s * 64 + g16 * 16);
#pragma unroll
          for (int t = 0; t < 4; ++t) d[t] = mfma(aa, bq[s * 4 + t], d[t]);
        }
        const float q0 = g16 == 0 ? d[0][0] : (g16 == 1 ? d[1][0] : (g16 == 2 ? d[2][0] : d[3][0]));
        const float q1 = g16 == 0 ? d[0][1] : (g16 == 1 ? d[1][1] : (g16 == 2 ? d[2][1] : d[3][1]));
        const bool odd = lane & 1;
        const float rcv = dpp<XOR1>(odd ? q0 : q1);
        publish(rs, WS_Q + ((((lane & 1) * 16 + h) * 4 + rq) * 128 + cbq * 32 + (lane >> 1)) * 16,
                __float_as_uint(odd ? rcv : q0), __float_as_uint(odd ? q1 : rcv), T);
      }
      STAMP(3);
      {
        int off[8]; v4u g[8];
#pragma unroll
        for (int k = 0; k < 8; ++k) off[k] = WS_Q + bh * 8192 + (k * 64 + lane) * 16;
        nap_until(now(), G_Q);
        gather<8, PSLP>(rs, off, g, T);
#pragma unroll
        for (int u = 0; u < 2; ++u) {
          const float qa = (__uint_as_float(g[u].x) + __uint_as_float(g[2 + u].x)) + (__uint_as_float(g[4 + u].x) + __uint_as_float(g[6 + u].x));
          const float qb = (__uint_as_float(g[u].z) + __uint_as_float(g[2 + u].z)) + (__uint_as_float(g[4 + u].z) + __uint_as_float(g[6 + u].z));
          *(LAS unsigned*)(L + L_Q + (u * 64 + lane) * 4) = pack2(qa, qb);
        }
        waitlds();
        if (lane == 0) flags[F_Q] = 1u;
      }
      STAMP(4);
      lds_wait(flags + F_SC, 8u);
      {
        int off[16]; v4u g[16];
#pragma unroll
        for (int k = 0; k < 16; ++k) off[k] = WS_SC + bh * 16384 + (((k ^ (kh * 8)) * 64) + lane) * 16;
        nap_until(now(), G_SC);
        gather<16, PSLP>(rs, off, g, T);
        STAMP(5);
        float m = -INFINITY;
#pragma unroll
        for (int k = 0; k < 16; ++k) m = fmaxf(fmaxf(fmaxf(lo16(g[k].x), hi16(g[k].x)), fmaxf(lo16(g[k].z), hi16(g[k].z))), m);
        m = wave_max(m);
        const float cs = 0.0625f * 1.4426950408889634f;
        const float mc = m * cs;
        float e[32]; float l = 0.f;
#pragma unroll
        for (int k = 0; k < 16; ++k) {
          const float e0_ = __builtin_amdgcn_exp2f(fmaf(lo16(g[k].x), cs, -mc)), e1_ = __builtin_amdgcn_exp2f(fmaf(hi16(g[k].x), cs, -mc));
          const float e2_ = __builtin_amdgcn_exp2f(fmaf(lo16(g[k].z), cs, -mc)), e3_ = __builtin_amdgcn_exp2f(fmaf(hi16(g[k].z), cs, -mc));
          l += (e0_ + e1_) + (e2_ + e3_);
          if (k < 8) { e[4 * k] = e0_; e[4 * k + 1] = e1_; e[4 * k + 2] = e2_; e[4 * k + 3] = e3_; }
        }
        l = wave_sum(l);
        const float rl = rcp_nr(l);
        const int t = lane & 15;
        const int half = t >> 3, j0 = (t >> 2) & 1, j1 = (t >> 1) & 1, j4 = t & 1;
#pragma unroll
        for (int k = 0; k < 8; ++k) {
          const int cq = k >> 1;
          const int wp = (k & 1) * 4 + (lane >> 4);
          const int wc = (j1 << 2) | (j0 << 1) | (wp >> 2), gg = ((wp & 3) << 1) | half, mm = (cq << 2) | (j4 << 1);
          *(LAS v2u*)(L + L_P + ((wc * 8 + gg) * 16 + mm) * 4) = v2u{pack2(e[4 * k] * rl, e[4 * k + 1] * rl), pack2(e[4 * k + 2] * rl, e[4 * k + 3] * rl)};
        }
        waitlds();
        if (lane == 0) flags[F_P] = 1u;
      }
      STAMP(6);
    } else if (a == 1) {
      lds_wait(flags + F_KI, 1u);
      const GAS unsigned char* src = (const GAS unsigned char*)p.wo + (size_t)(h * 256 + dq * 64) * (D * 2) + lane * 16;
#pragma unroll
#define NOWO 0
#define POL_WO POL_W
      for (int i = 0; i < (NOWO ? 0 : 56); ++i) {
        if (i >= WDEPTH) waitvm<WDEPTH - 1>();
        __builtin_amdgcn_global_load_lds((const GAS unsigned*)(src + i * 1024), (LAS unsigned*)(L + L_WO + i * 1024), 16, 0, POL_WO);
      }
      waitvm<0>();
      if (lane == 0) flags[F_WO] = 1u;
      STAMP(2);
    }
    if (a == 3 || (DUALP && a == 2)) {
      if (is_red) {
        const unsigned xr = __builtin_amdgcn_raw_buffer_load_b32(rx, (rb * D + rchunk * 8 + (lane & 3) * 2) * 2, 0, 0);
        int off[2] = {wsmod + (D + rchunk * 4 + (lane & 3)) * 16, wsmod + (5 * D / 2 + rchunk * 4 + (lane & 3)) * 16}; v4u g[2];
        lds_wait(flags + F_KI, 1u);
        gather<2, 32>(rs, off, g, T);
        kp[0] = rb ? hi16(g[0].x) : lo16(g[0].x); kp[1] = rb ? hi16(g[0].z) : lo16(g[0].z);
        kp[2] = rb ? hi16(g[1].x) : lo16(g[1].x); kp[3] = rb ? hi16(g[1].z) : lo16(g[1].z);
        kp[4] = lo16(xr); kp[5] = hi16(xr);
        STAMP(2);
      }
    }
    if (is_ffn) {
      const int col2 = ((a & 1) * 56 + l56) * 2;
      int off[4] = {wsmod + (2 * D + col2) * 16, wsmod + (2 * D + col2 + 1) * 16, wsmod + (3 * D / 2 + col2) * 16, wsmod + (3 * D / 2 + col2 + 1) * 16};
      v4u g[4];
      lds_wait(flags + F_KI, 1u);
      gather<4, 32>(rs, off, g, T);
      const bool hb = a >> 1;
      kp[0] = rbf(1.f + (hb ? hi16(g[0].x) : lo16(g[0].x))); kp[1] = rbf(1.f + (hb ? hi16(g[0].z) : lo16(g[0].z)));
      kp[2] = rbf(1.f + (hb ? hi16(g[1].x) : lo16(g[1].x))); kp[3] = rbf(1.f + (hb ? hi16(g[1].z) : lo16(g[1].z)));
      kp[4] = hb ? hi16(g[2].x) : lo16(g[2].x); kp[5] = hb ? hi16(g[2].z) : lo16(g[2].z);
      kp[6] = hb ? hi16(g[3].x) : lo16(g[3].x); kp[7] = hb ? hi16(g[3].z) : lo16(g[3].z);
    }
#pragma unroll
    for (int s = 0; s < 8; ++s) bw[s] = v4u{0u, 0u, 0u, 0u};
    if (a == 0) {
      lds_wait(flags + F_WO, 1u);
#pragma unroll
      for (int s = 0; s < 8; ++s) bw[s] = bfrag(L + L_WO + (32 * (s & 1) + tr_row) * (D * 2) + (24 + (s >> 1)) * 32 + tr_col, D * 2);
    }
    STAMP(7);
  }

  const int n16 = lane & 15;
  const int tsel = g16 == 0 ? t0 : (g16 == 2 ? t1 : t2);
  if (wave == 0 || wave >= 4) {
    lds_spin(flags + F_O, 8u);
    const v4u a0 = *(const LAS v4u*)(L + L_OA + n16 * 144 + g16 * 16), a1 = *(const LAS v4u*)(L + L_OA + n16 * 144 + 64 + g16 * 16);
    const v4f z = {0.f, 0.f, 0.f, 0.f};
    const v4f d0 = mfma(a1, bw[1], mfma(a0, bw[0], z)), d1 = mfma(a1, bw[3], mfma(a0, bw[2], z));
    const v4f d2 = mfma(a1, bw[5], mfma(a0, bw[4], z)), d3 = mfma(a1, bw[7], mfma(a0, bw[6], z));
    const float u1 = tr32((d0[0] + d0[1]) + (d0[2] + d0[3]), (d2[0] + d2[1]) + (d2[2] + d2[3]));
    const float u2 = tr32((d1[0] + d1[1]) + (d1[2] + d1[3]), (d3[0] + d3[1]) + (d3[2] + d3[3]));
    const float v = tr16(u1, u2);
    const float vn = dpp<XOR1>(v);
    const int n = (w0 + g16 * wst) * 16 + n16;
    if ((lane & 1) == 0 && (g16 < 3 || wave == 0))
      publish(rs, WS_P1 + (((b * 56 + (n >> 3)) * 128 + h * 8 + c) * 4 + ((n >> 1) & 3)) * 16, __float_as_uint(v), __float_as_uint(vn), T);
    if (lane == 0) lds_inc(flags + F_PUB);
    STAMP(8);
  }
  if (L2WARM && !is_ffn && wave >= 4 && wave < 8) {
    const int slot = (q3 ? 17 + (li >> 2) : jn) * 4 + wave - 4;
    const int ln = slot * 64 + lane;
    const GAS unsigned char* a_ = ln < 3584 ? (const GAS unsigned char*)((ln & 4) ? p.wu : p.wg) + (size_t)(ln >> 3) * (F * 2) + ((xcd * 448) & ~127) + (ln & 3) * 128
                                            : (const GAS unsigned char*)p.wd + (size_t)xcd * 7 * 32 * (D * 2) + (size_t)(ln < 5152 ? ln - 3584 : 0) * 128;
    const unsigned t_ = *(const GAS unsigned*)a_;
    asm volatile("" ::"v"(t_));
  }
  if (is_red) {
    if (wave == 3 || (DUALP && wave == 2)) {
      float x20, x21;
      lds_wait(flags + F_PUB, 9u);
      if (wave == 2) __builtin_amdgcn_s_sleep(DUALP);
      {
        int off[8]; v4u g[8];
#pragma unroll
        for (int k = 0; k < 8; ++k) off[k] = WS_P1 + (rb * 56 + rchunk) * 8192 + (k * 64 + lane) * 16;
        gather<8, 1>(rs, off, g, T);
        STAMP(9);
        float s0 = 0.f, s1 = 0.f;
#pragma unroll
        for (int k = 0; k < 8; k += 2) { s0 += __uint_as_float(g[k].x) + __uint_as_float(g[k + 1].x); s1 += __uint_as_float(g[k].z) + __uint_as_float(g[k + 1].z); }
        s0 += dpp<ROR4>(s0); s1 += dpp<ROR4>(s1); s0 += dpp<ROR8>(s0); s1 += dpp<ROR8>(s1);
        s0 = sum16(s0); s1 = sum16(s1); s0 = sum32(s0); s1 = sum32(s1);
        x20 = rbf(kp[4] + rbf(kp[0] * rbf(s0))); x21 = rbf(kp[5] + rbf(kp[1] * rbf(s1)));
        const unsigned xp = pack2(x20, x21), xq = dppu<XOR1>(xp);
        float sm = x20 + x21; sm += dpp<XOR1>(sm); sm += dpp<XOR2>(sm);
        const float d0 = x20 - sm * 0.125f, d1 = x21 - sm * 0.125f;
        float m2 = fmaf(d0, d0, d1 * d1); m2 += dpp<XOR1>(m2); m2 += dpp<XOR2>(m2);
        const bool stl = (lane & 3) == 1;
        if ((lane & 3) < 3 && lane < 4 * NREP)
          publish(rs, (lane >> 2) * XS_REP + (stl ? WS_ST + (rb * 56 + rchunk) * 16 : WS_X2 + (rb * 112 + rchunk * 2 + ((lane >> 1) & 1)) * 16),
                  stl ? __float_as_uint(sm) : xp, stl ? __float_as_uint(m2) : xq, T);
        STAMP(10);
      }
      {
        int off[4]; v4u g[4];
#pragma unroll
        for (int k = 0; k < 4; ++k) { int pc = k * 64 + lane; pc = pc < 224 ? pc : pc - 32; off[k] = WS_P2 + (rb * 56 + rchunk) * (NF * 64) + pc * 16; }
        if (wave == 2) __builtin_amdgcn_s_sleep(DUALP);
        gather<4, 1>(rs, off, g, T);
        float s0 = (__uint_as_float(g[0].x) + __uint_as_float(g[1].x)) + (__uint_as_float(g[2].x) + (lane < 32 ? __uint_as_float(g[3].x) : 0.f));
        float s1 = (__uint_as_float(g[0].z) + __uint_as_float(g[1].z)) + (__uint_as_float(g[2].z) + (lane < 32 ? __uint_as_float(g[3].z) : 0.f));
        s0 += dpp<ROR4>(s0); s1 += dpp<ROR4>(s1); s0 += dpp<ROR8>(s0); s1 += dpp<ROR8>(s1);
        s0 = sum16(s0); s1 = sum16(s1); s0 = sum32(s0); s1 = sum32(s1);
        const unsigned ov = pack2(x20 + rbf(kp[2] * rbf(s0)), x21 + rbf(kp[3] * rbf(s1)));
        if (lane < 4) ((GAS unsigned*)p.out)[(rb * D + rchunk * 8) / 2 + lane] = ov;
        if (bid == 0 && lane == 0) __builtin_amdgcn_raw_buffer_store_b32(T, rs, WS_EPOCH, 0, AUX_SC1);
        STAMP(11);
      }
    }
  } else if (is_ffn) {
    {
      if (wave >= 1 && wave < 4) lds_wait(flags + F_PUB, 9u);
      const GAS unsigned char* gsrc = (const GAS unsigned char*)((lane & 4) ? p.wu : p.wg) + (size_t)(lane >> 3) * (F * 2) + fidx * 64 + (lane & 3) * 16;
      const GAS unsigned char* dsrc = (const GAS unsigned char*)p.wd + (size_t)fidx * 32 * (D * 2) + lane * 16;
#pragma unroll
      for (int i = 0; i < 7; ++i) {
        const int u = i * 12 + wave;
        const GAS unsigned char* src = u < 56 ? gsrc + (size_t)u * 8 * (F * 2) : dsrc + (u - 56) * 1024;
        glds16(src, (unsigned)(size_t)(L + L_GU) + u * 1024);
      }
      asm volatile("s_waitcnt vmcnt(2)" ::: "memory");
      if (wave < 4) STAMP(1);
      bar();
    }
    v4u bd[3];
    if (wave < 4) {
      v4u bg[14];
      {
        LAS unsigned char* gq = L + L_GU + tr_row * 128 + (lane & 2) * 32 + wave * 16 + (lane & 1) * 8;
#pragma unroll
        for (int s = 0; s < 14; ++s) bg[s] = bfrag(gq + s * 32 * 128, 128);
      }
      const int l56 = lane < 56 ? lane : 55, xbt = wave >> 1;
      {
        int off[2] = {WS_X2 + xcd * XS_REP + (wave * 56 + l56) * 16, WS_ST + xcd * XS_REP + (xbt * 56 + l56) * 16}; v4u g[2];
        gather<2, 1>(rs, off, g, T);
        STAMP(9);
        const float sm = lane < 56 ? __uint_as_float(g[1].x) : 0.f;
        const float mean = wave_sum(sm) * (1.f / D);
        const float dm = sm * 0.125f - mean;
        const float m2 = wave_sum(lane < 56 ? fmaf(8.f * dm, dm, __uint_as_float(g[1].z)) : 0.f);
        const float rsd = ln_rsd(m2 * (1.f / D)), mbf = rbf(mean);
        const float h0 = rbf(rbf(rbf(lo16(g[0].x) - mbf) * rsd) * kp[0]) + kp[4], h1 = rbf(rbf(rbf(hi16(g[0].x) - mbf) * rsd) * kp[1]) + kp[5];
        const float h2 = rbf(rbf(rbf(lo16(g[0].z) - mbf) * rsd) * kp[2]) + kp[6], h3 = rbf(rbf(rbf(hi16(g[0].z) - mbf) * rsd) * kp[3]) + kp[7];
        *(LAS v2u*)(L + L_H2 + (wave * 56 + l56) * 8) = v2u{pack2(h0, h1), pack2(h2, h3)};
      }
      asm volatile("s_waitcnt vmcnt(0)" ::: "memory");
      bar();
      bd[0] = bfrag(L + L_DN + tr_row * (D * 2) + t0 * 32 + tr_col, D * 2);
      bd[1] = bfrag(L + L_DN + tr_row * (D * 2) + t1 * 32 + tr_col, D * 2);
      bd[2] = bfrag(L + L_DN + tr_row * (D * 2) + t2 * 32 + tr_col, D * 2);
      v4f da = {0.f, 0.f, 0.f, 0.f}, db = {0.f, 0.f, 0.f, 0.f};
#pragma unroll
      for (int s = 0; s < 14; s += 2) {
        const v4u a0 = *(const LAS v4u*)(L + L_H2 + (lane & 1) * (D * 2) + s * 64 + g16 * 16);
        const v4u a1 = *(const LAS v4u*)(L + L_H2 + (lane & 1) * (D * 2) + s * 64 + 64 + g16 * 16);
        da = mfma(a0, bg[s], da); db = mfma(a1, bg[s + 1], db);
      }
      const float g0 = rbf(da[0] + db[0]), g1 = rbf(da[1] + db[1]);
      const float u0 = dpp<ROR8>(g0), u1 = dpp<ROR8>(g1);
      const float k1 = -2.f * 0.7978845608028654f * 1.4426950408889634f;
      const float e0_ = __builtin_amdgcn_exp2f(k1 * fmaf(0.044715f * g0 * g0, g0, g0)), e1_ = __builtin_amdgcn_exp2f(k1 * fmaf(0.044715f * g1 * g1, g1, g1));
      const float ge0 = rbf(g0 * __builtin_amdgcn_rcpf(1.f + e0_)), ge1 = rbf(g1 * __builtin_amdgcn_rcpf(1.f + e1_));
      const unsigned av = pack2(ge0 * u0, ge1 * u1);
      if (lane < 8) {
        *(LAS u16*)(L + L_ACT + (wave * 8 + lane) * 2) = (u16)av;
        *(LAS u16*)(L + L_ACT + 64 + (wave * 8 + lane) * 2) = (u16)(av >> 16);
      }
      STAMP(10);
    } else {
      asm volatile("s_waitcnt vmcnt(0)" ::: "memory");
      bar();
      bd[0] = bfrag(L + L_DN + tr_row * (D * 2) + t0 * 32 + tr_col, D * 2);
      bd[1] = bfrag(L + L_DN + tr_row * (D * 2) + t1 * 32 + tr_col, D * 2);
      bd[2] = bfrag(L + L_DN + tr_row * (D * 2) + t2 * 32 + tr_col, D * 2);
    }
    bar();
    {
      const v4u aa = *(const LAS v4u*)(L + L_ACT + (lane & 1) * 64 + g16 * 16);
      const v4f z = {0.f, 0.f, 0.f, 0.f};
      const v4f d0 = mfma(aa, bd[0], z), d1 = mfma(aa, bd[1], z), d2 = mfma(aa, bd[2], z);
      const float v0 = g16 == 0 ? d0[0] : (g16 == 2 ? d1[0] : d2[0]), v1 = g16 == 0 ? d0[1] : (g16 == 2 ? d1[1] : d2[1]);
      const bool odd = lane & 1;
      const float rcv = dpp<XOR1>(odd ? v0 : v1);
      const int n = tsel * 16 + n16;
      if ((g16 & 1) == 0 || (g16 == 1 && wave < 4))
        publish(rs, WS_P2 + ((((lane & 1) * 56 + (n >> 3)) * NF + fidx) * 4 + ((n >> 1) & 3)) * 16,
                __float_as_uint(odd ? rcv : v0), __float_as_uint(odd ? v1 : rcv), T);
      STAMP(11);
    }
  }
  STAMP_FLUSH();
}
}

extern "C" void kernel_launch(void* const* d_in, const int* in_sizes, int n_in, void* d_out, int out_size, void* d_ws,
                              size_t ws_size, hipStream_t stream) {
  Params p;
  p.x = (const u16*)d_in[0]; p.kc = (const u16*)d_in[1]; p.vc = (const u16*)d_in[2];
  p.wq = (const u16*)d_in[3]; p.wo = (const u16*)d_in[4];
  p.wg = (const u16*)d_in[6]; p.wu = (const u16*)d_in[7]; p.wd = (const u16*)d_in[8];
  p.c = (const u16*)d_in[9]; p.wmod = (const u16*)d_in[10];
  p.out = (u16*)d_out; p.ws = (unsigned char*)d_ws;
  (void)hipFuncSetAttribute((const void*)fused_block, hipFuncAttributeMaxDynamicSharedMemorySize, L_END);
  hipLaunchKernelGGL(fused_block, dim3(256), dim3(NTHR), L_END, stream, p);
}
```

```cpp
#include <hip/hip_runtime.h>
#include <stdint.h>

typedef unsigned short u16;
typedef unsigned v4u __attribute__((ext_vector_type(4)));
typedef unsigned v2u __attribute__((ext_vector_type(2)));
typedef float v4f __attribute__((ext_vector_type(4)));
typedef float v2f __attribute__((ext_vector_type(2)));
typedef short v8s __attribute__((ext_vector_type(8)));
typedef short v4s __attribute__((ext_vector_type(4)));
typedef __bf16 bf16x2 __attribute__((ext_vector_type(2)));
#define GAS __attribute__((address_space(1)))
#define LAS __attribute__((address_space(3)))

#define KDEPTH 3
#define VDEPTH 3
#define WDEPTH 8
#define PSTART 26
#define G_MOD 450
#define G_Q 150
#define G_SC 100
#define TBIG 16
#define TTHR 64
#define G_MP 60
#define PRETOUCH 1
#define VEARLY 8
#define HOLDL 0
#define EFROM 32
#define EDEPTH 6
#define FSTART 31
#define DUALP 0
#define L2WARM 1
#define QSHIFT 1
#define G_Q3 1000
#define CODEWARM 1
#define PTHR 38
#define HOLDK 0
#define PSLP 24
#define POL_K 2
#define POL_V 2
#define POL_W 2
#define DBG_STAMP 0

namespace {
constexpr int D = 448, NH = 16, SK = 4096, DH = 256, F = 1792;
constexpr int NTHR = 768;
constexpr int NF = 56;
constexpr int AUX_NT = 2, AUX_SC1 = 16;
constexpr unsigned SPIN_CAP = 1u << 16;

constexpr int WS_EPOCH = 0;
constexpr int NREP = 8;
constexpr int MOD_REP = 21504, XS_REP = 5376;
constexpr int WS_MOD = 256;
constexpr int WS_X2 = WS_MOD + NREP * MOD_REP;
constexpr int WS_ST = WS_X2 + 3584;
constexpr int WS_Q = 262144;
static_assert(WS_X2 + NREP * XS_REP <= WS_Q, "ws map");
constexpr int WS_SC = WS_Q + 32 * 8192;
constexpr int WS_P1 = WS_SC + 32 * 16384;
constexpr int WS_P2 = WS_P1 + 2 * 56 * 8192;
constexpr int WS_MP = WS_P2 + 2 * 56 * NF * 64;
constexpr int WS_END = WS_MP + 42 * 3 * 1024;
constexpr int WS_STAMP = 4 << 20;
static_assert(WS_END <= WS_STAMP, "ws map");

constexpr int L_WO = 0;
constexpr int L_GU = 57344;
constexpr int L_DN = L_GU + 57344;
constexpr int L_Q = L_DN + 28672;
constexpr int L_H1 = L_Q + 512;
constexpr int L_P = L_H1 + 1024;
constexpr int L_OA = L_P + 4096;
constexpr int L_H2 = L_OA + 2304;
constexpr int L_WM = L_GU;
constexpr int L_VE = L_GU;
constexpr int L_WQ = L_DN;
constexpr int L_ACT = L_H2 + 1792;
constexpr int L_FLAG = L_ACT + 128;
constexpr int L_LAT = L_FLAG + 64;
constexpr int L_END = L_LAT + 32;
static_assert(L_END <= 160 * 1024 && L_END > 80 * 1024, "lds");
enum { F_Q = 0, F_P = 1, F_WO = 2, F_SC = 3, F_MOD = 4, F_KI = 5, F_TV = 6, F_U = 7, F_T = 8, F_PUB = 9, F_O = 10 };

struct Params {
  const u16 *x, *kc, *vc, *wq, *wo, *wg, *wu, *wd, *c, *wmod;
  u16* out;
  unsigned char* ws;
};

__device__ __forceinline__ float lo16(unsigned w) { return __uint_as_float(w << 16); }
__device__ __forceinline__ float hi16(unsigned w) { return __uint_as_float(w & 0xffff0000u); }
__device__ __forceinline__ unsigned pack2(float a, float b) {
  bf16x2 v = {(__bf16)a, (__bf16)b};
  return __builtin_bit_cast(unsigned, v);
}
__device__ __forceinline__ v2f unpk(unsigned w) { return v2f{lo16(w), hi16(w)}; }
__device__ __forceinline__ float rbf(float f) { return lo16(pack2(f, f)); }
__device__ __forceinline__ float dot2(unsigned a, unsigned b, float c) {
  return __builtin_amdgcn_fdot2_f32_bf16(__builtin_bit_cast(bf16x2, a), __builtin_bit_cast(bf16x2, b), c, false);
}
__device__ __forceinline__ float rcp_nr(float x) { const float r = __builtin_amdgcn_rcpf(x); return fmaf(fmaf(-x, r, 1.f), r, r); }
template <int N> __device__ __forceinline__ void waitvm() {
  asm volatile("" ::: "memory");
  __builtin_amdgcn_s_waitcnt((N & 15) | ((N >> 4) << 14) | 0x0F70);
  asm volatile("" ::: "memory");
}
__device__ __forceinline__ void waitlds() { asm volatile("s_waitcnt lgkmcnt(0)" ::: "memory"); }
__device__ __forceinline__ void bar() {
  __builtin_amdgcn_sched_barrier(0);
  asm volatile("s_waitcnt lgkmcnt(0)" ::: "memory");
  __builtin_amdgcn_s_barrier();
  asm volatile("" ::: "memory");
  __builtin_amdgcn_sched_barrier(0);
}
template <int CTRL> __device__ __forceinline__ float dpp(float x) {
  return __builtin_bit_cast(float, __builtin_amdgcn_mov_dpp(__builtin_bit_cast(int, x), CTRL, 0xf, 0xf, true));
}
template <int CTRL> __device__ __forceinline__ unsigned dppu(unsigned x) {
  return (unsigned)__builtin_amdgcn_mov_dpp((int)x, CTRL, 0xf, 0xf, true);
}
constexpr int XOR1 = 0xB1, XOR2 = 0x4E, XOR7 = 0x141, ROR8 = 0x128, ROR4 = 0x124;
__device__ __forceinline__ float tr32(float a, float b) {
  auto r = __builtin_amdgcn_permlane32_swap(__float_as_uint(a), __float_as_uint(b), false, false);
  return __uint_as_float(r[0]) + __uint_as_float(r[1]);
}
__device__ __forceinline__ float tr16(float a, float b) {
  auto r = __builtin_amdgcn_permlane16_swap(__float_as_uint(a), __float_as_uint(b), false, false);
  return __uint_as_float(r[0]) + __uint_as_float(r[1]);
}
template <int CTRL> __device__ __forceinline__ float trd(float a, float b, bool bit) {
  const float t1 = a + dpp<CTRL>(a), t2 = b + dpp<CTRL>(b);
  return bit ? t2 : t1;
}
__device__ __forceinline__ float sum32(float a) { return tr32(a, a); }
__device__ __forceinline__ float sum16(float a) { return tr16(a, a); }
__device__ __forceinline__ float wave_sum(float v) {
  v += dpp<XOR1>(v); v += dpp<XOR2>(v); v += dpp<XOR7>(v); v += dpp<ROR8>(v);
  v = sum16(v); v = sum32(v);
  return v;
}
__device__ __forceinline__ float wave_max(float v) {
  v = fmaxf(v, dpp<XOR1>(v)); v = fmaxf(v, dpp<XOR2>(v)); v = fmaxf(v, dpp<XOR7>(v)); v = fmaxf(v, dpp<ROR8>(v));
  auto r = __builtin_amdgcn_permlane16_swap(__float_as_uint(v), __float_as_uint(v), false, false);
  v = fmaxf(__uint_as_float(r[0]), __uint_as_float(r[1]));
  auto t = __builtin_amdgcn_permlane32_swap(__float_as_uint(v), __float_as_uint(v), false, false);
  return fmaxf(__uint_as_float(t[0]), __uint_as_float(t[1]));
}
__device__ __forceinline__ v4f mfma(v4u a, v4u b, v4f c) {
  return __builtin_amdgcn_mfma_f32_16x16x32_bf16(__builtin_bit_cast(v8s, a), __builtin_bit_cast(v8s, b), c, 0, 0, 0);
}
__device__ __forceinline__ v4u bfrag(LAS unsigned char* q, int stride) {
  const v2u a = __builtin_bit_cast(v2u, __builtin_amdgcn_ds_read_tr16_b64_v4i16((LAS v4s*)q));
  const v2u b = __builtin_bit_cast(v2u, __builtin_amdgcn_ds_read_tr16_b64_v4i16((LAS v4s*)(q + 4 * stride)));
  return v4u{a.x, a.y, b.x, b.y};
}

__device__ __forceinline__ void glds16(const GAS unsigned char* gsrc, unsigned lds_dst) {
  unsigned keep;
  asm volatile("s_mov_b32 %0, m0\n\ts_mov_b32 m0, %2\n\ts_nop 0\n\tglobal_load_lds_dwordx4 %1, off\n\ts_mov_b32 m0, %0"
               : "=&s"(keep) : "v"(gsrc), "s"(lds_dst) : "memory");
}
__device__ __forceinline__ void glds16_nt(const GAS unsigned char* gsrc, unsigned lds_dst) {
  unsigned keep;
  asm volatile("s_mov_b32 %0, m0\n\ts_mov_b32 m0, %2\n\ts_nop 0\n\tglobal_load_lds_dwordx4 %1, off nt\n\ts_mov_b32 m0, %0"
               : "=&s"(keep) : "v"(gsrc), "s"(lds_dst) : "memory");
}
typedef __amdgpu_buffer_rsrc_t rsrc_t;
__device__ __forceinline__ v4u ldstream(rsrc_t r, int voff, int soff, bool keep) {
  if (keep) return __builtin_amdgcn_raw_buffer_load_b128(r, voff, soff, 0);
  return __builtin_amdgcn_raw_buffer_load_b128(r, voff, soff, AUX_NT);
}
__device__ __forceinline__ rsrc_t mkrsrc(const void* p, int bytes) { return __builtin_amdgcn_make_buffer_rsrc((void*)p, 0, bytes, 0x00020000); }
__device__ __forceinline__ void publish(rsrc_t rs, int off, unsigned a, unsigned b, unsigned T) {
  v4u v = {a, T, b, T};
  __builtin_amdgcn_raw_buffer_store_b128(v, rs, off, 0, AUX_SC1);
}
template <int N, int SLP> __device__ __forceinline__ void gather(rsrc_t rs, const int (&off)[N], v4u (&v)[N], unsigned T) {
#pragma unroll
  for (int k = 0; k < N; ++k) v[k] = __builtin_amdgcn_raw_buffer_load_b128(rs, off[k], 0, AUX_SC1);
  for (unsigned spins = 0; spins < SPIN_CAP; ++spins) {
    bool ok = true;
#pragma unroll
    for (int k = 0; k < N; ++k) ok &= (v[k].y == T) & (v[k].w == T);
    const int miss = __builtin_popcountll(__builtin_amdgcn_ballot_w64(!ok));
    if (miss == 0) break;
    if (SLP < TBIG && miss > TTHR) __builtin_amdgcn_s_sleep(TBIG); else __builtin_amdgcn_s_sleep(SLP);
    asm volatile("" ::: "memory");
#pragma unroll
    for (int k = 0; k < N; ++k)
      if ((v[k].y != T) | (v[k].w != T)) v[k] = __builtin_amdgcn_raw_buffer_load_b128(rs, off[k], 0, AUX_SC1);
  }
}
__device__ __forceinline__ void lds_wait(volatile LAS unsigned* f, unsigned want) {
  for (unsigned spins = 0; spins < (1u << 22); ++spins) {
    if (__builtin_amdgcn_readfirstlane(*f) >= want) break;
    __builtin_amdgcn_s_sleep(1);
  }
  asm volatile("" ::: "memory");
}
__device__ __forceinline__ void lds_spin(volatile LAS unsigned* f, unsigned want) {
  for (unsigned spins = 0; spins < (1u << 24); ++spins)
    if (__builtin_amdgcn_readfirstlane(*f) >= want) break;
  asm volatile("" ::: "memory");
}
__device__ __forceinline__ unsigned now() { return (unsigned)__builtin_amdgcn_s_memrealtime(); }
__device__ __forceinline__ void nap_until(unsigned t0, unsigned ticks) {
  for (int i = 0; i < 4096 && now() - t0 < ticks; ++i) __builtin_amdgcn_s_sleep(4);
}
__device__ __forceinline__ void lds_inc(volatile LAS unsigned* f) {
  __hip_atomic_fetch_add((LAS unsigned*)f, 1u, __ATOMIC_RELAXED, __HIP_MEMORY_SCOPE_WORKGROUP);
}
constexpr int pairs_done(int i) {
  if (i < PSTART) return 0;
  int a = (i - VDEPTH + 1) / 2, b = (i - PSTART + 1) * 3;
  if (i >= EFROM) { const int e = (i - EDEPTH + 1) / 2, n = (EFROM - VDEPTH) / 2; a = e > n ? e : n; }
  return a < b ? a : b;
}
__device__ __forceinline__ float ln_rsd(float var) { return rcp_nr(rbf(__builtin_amdgcn_sqrtf(rbf(rbf(var) + 1e-5f)))); }

#define STAMP(i)
#define STAMP_FLUSH()

__global__ void __launch_bounds__(NTHR, 3) fused_block(Params p) {
  extern __shared__ __attribute__((aligned(16))) unsigned char smem[];
  LAS unsigned char* L = (LAS unsigned char*)smem;
  volatile LAS unsigned* flags = (volatile LAS unsigned*)(L + L_FLAG);
  const int bid = blockIdx.x, tid = threadIdx.x, lane = tid & 63;
  const int wave = __builtin_amdgcn_readfirstlane(tid >> 6);
  const int xcd = bid & 7, li = bid >> 3;
#define CROT 0
  const int cu = xcd * 32 + ((li + CROT) & 31);
  const int h = cu >> 4, b = (cu >> 3) & 1, c = cu & 7, kh = c >> 2, dq = c & 3, bh = b * 16 + h;
  const bool q3 = (li & 3) == 3;
  const int jn = (li >> 2) * 3 + (li & 3);
  const int mu = q3 ? 136 + (li >> 2) * 8 + xcd : jn * 8 + xcd;
  const int mb = mu >> 2, mpart = mu & 3;
  const bool is_mod = q3 ? li < 16 : jn < 17, is_ffn = !q3 && jn >= 17, is_red = !q3 && jn < 14;
  const int fidx = xcd * 7 + jn - 17;
  const int rb = jn & 1, rchunk = xcd * 7 + (jn >> 1);
  asm volatile("" ::"s"(p.x), "s"(p.kc), "s"(p.vc), "s"(p.wq), "s"(p.wo), "s"(p.c), "s"(p.wmod), "s"(p.ws));
  const rsrc_t rs = mkrsrc(p.ws, WS_END);
  STAMP(0);
  const unsigned tstart = now();
  unsigned e0 = 0u;
  if (wave == 0) e0 = __builtin_amdgcn_raw_buffer_load_b32(rs, WS_EPOCH, 0, AUX_SC1);
  if (tid < 16) flags[tid] = 0u;
  unsigned T;
  v4u bw[8];
  float kp[8];
#pragma unroll
  for (int j = 0; j < 8; ++j) kp[j] = 0.f;
  const int t0 = wave < 4 ? 16 + wave : wave - 4, t1 = wave < 4 ? 20 + wave : wave + 4, t2 = wave < 4 ? 24 + wave : 0;
  const int w0 = wave < 4 ? 24 : wave - 4, wst = wave < 4 ? 1 : 8;
  const int tr_row = 8 * (lane >> 4) + ((lane >> 2) & 3), tr_col = (lane & 3) * 8, g16 = lane >> 4;

  if (wave >= 4) {
    const int sw = wave - 4;
    const rsrc_t rk = mkrsrc(p.kc, 64 << 20), rv = mkrsrc(p.vc, 64 << 20);
    const int koff = (bh * SK + c * 512) * 512 + sw * 1024 + lane * 16;
    const int voff = (bh * SK + kh * 2048 + sw * 8 + (lane >> 3)) * 512 + dq * 128 + (lane & 7) * 16;
    v4u kb[32], vb[32];
    constexpr bool keepk = false, keepv = false;
    if (bid < HOLDK) { bar(); lds_wait(flags + F_MOD, (mpart == 0 && HOLDL) ? 2u : 1u); }
#pragma unroll
    for (int i = 0; i < 32; ++i) {
      if (i == KDEPTH && bid >= HOLDK) bar();
      if (i >= KDEPTH) waitvm<KDEPTH - 1>();
      kb[i] = ldstream(rk, koff, i * 8192, keepk);
      __builtin_amdgcn_sched_barrier(0);
    }
    if (sw == 0 && lane == 0) flags[F_KI] = 1u;
    STAMP(1);
    lds_wait(flags + F_U, 3u);
    const GAS unsigned char* vsrc = (const GAS unsigned char*)p.vc + voff;
    const unsigned vdst = (unsigned)(size_t)(L + L_VE) + sw * 1024;
    int ev = 0;
    while (ev < VEARLY && __builtin_amdgcn_readfirstlane(flags[F_Q]) == 0u) {
      waitvm<VDEPTH - 1>();
      glds16_nt(vsrc + ev * 32768, vdst + ev * 8192);
      ++ev;
    }
    lds_wait(flags + F_Q, 1u);
    STAMP(2);
    const v4u qv = *(const LAS v4u*)(L + L_Q + (lane & 31) * 16);
    float s2[16];
#pragma unroll
    for (int m = 0; m < 16; ++m) {
      float a = 0.f, bb = 0.f;
#pragma unroll
      for (int j = 0; j < 4; ++j) { a = dot2(kb[2 * m][j], qv[j], a); bb = dot2(kb[2 * m + 1][j], qv[j], bb); }
      s2[m] = tr16(a, bb);
      if ((m & 3) == 3) {
        __builtin_amdgcn_sched_barrier(0);
        if (ev < VEARLY) { waitvm<VDEPTH - 1>(); glds16_nt(vsrc + ev * 32768, vdst + ev * 8192); ++ev; }
        waitvm<VDEPTH - 1>();
        vb[VEARLY + (m >> 2)] = ldstream(rv, voff, (VEARLY + (m >> 2)) * 32768, keepv);
        __builtin_amdgcn_sched_barrier(0);
      }
    }
    float s4[8], s8[4], s16[2];
#pragma unroll
    for (int u = 0; u < 8; ++u) s4[u] = trd<ROR8>(s2[2 * u], s2[2 * u + 1], lane & 8);
#pragma unroll
    for (int u = 0; u < 4; ++u) s8[u] = trd<XOR7>(s4[u], s4[u + 4], lane & 4);
#pragma unroll
    for (int u = 0; u < 2; ++u) s16[u] = trd<XOR2>(s8[u], s8[u + 2], lane & 2);
    const float sc = trd<XOR1>(s16[0], s16[1], lane & 1);
    T = __builtin_amdgcn_readfirstlane(flags[F_T]);
    {
      const unsigned pr = pack2(sc, dpp<XOR1>(sc));
      const unsigned pr2 = dppu<XOR2>(pr);
      if ((lane & 3) == 0) publish(rs, WS_SC + bh * 16384 + ((c * 8 + sw) * 16 + (lane >> 2)) * 16, pr, pr2, T);
      if (lane == 0) lds_inc(flags + F_SC);
    }
    STAMP(3);
    while (ev < VEARLY) { waitvm<VDEPTH - 1>(); glds16_nt(vsrc + ev * 32768, vdst + ev * 8192); ++ev; }
    float acc[8];
#pragma unroll
    for (int j = 0; j < 8; ++j) acc[j] = 0.f;
    v4u pp[4];
#pragma unroll
    for (int i = 4 + VEARLY; i <= 32; ++i) {
      if (i < 32) {
        __builtin_amdgcn_sched_barrier(0);
        if (i >= EFROM) waitvm<EDEPTH - 1>(); else waitvm<VDEPTH - 1>();
        vb[i] = ldstream(rv, voff, i * 32768, keepv);
        __builtin_amdgcn_sched_barrier(0);
      }
      if (i == PSTART) {
        STAMP(4);
        lds_wait(flags + F_P, 1u);
        STAMP(5);
#pragma unroll
        for (int u = 0; u < 4; ++u) pp[u] = *(const LAS v4u*)(L + L_P + (sw * 8 + (lane >> 3)) * 64 + u * 16);
      }
      if (i == 31) {
        lds_wait(flags + F_WO, 1u);
#pragma unroll
        for (int s = 0; s < 2; ++s) {
          bw[s] = bfrag(L + L_WO + (32 * s + tr_row) * (D * 2) + w0 * 32 + tr_col, D * 2);
          bw[2 + s] = bfrag(L + L_WO + (32 * s + tr_row) * (D * 2) + (w0 + 8) * 32 + tr_col, D * 2);
          bw[4 + s] = bfrag(L + L_WO + (32 * s + tr_row) * (D * 2) + (w0 + 16) * 32 + tr_col, D * 2);
          bw[6 + s] = v4u{0u, 0u, 0u, 0u};
        }
      }
      const int m0 = pairs_done(i - 1), m1 = i < 32 ? pairs_done(i) : 16;
#pragma unroll
      for (int m = m0; m < m1; ++m) {
        const unsigned pw = pp[m >> 2][m & 3];
        const v4u va = 2 * m < VEARLY ? *(const LAS v4u*)(L + L_VE + (2 * m * 8 + sw) * 1024 + lane * 16) : vb[2 * m];
        const v4u vc2 = 2 * m + 1 < VEARLY ? *(const LAS v4u*)(L + L_VE + ((2 * m + 1) * 8 + sw) * 1024 + lane * 16) : vb[2 * m + 1];
#pragma unroll
        for (int j = 0; j < 4; ++j) {
          const unsigned a = va[j], bb = vc2[j];
          acc[2 * j] = dot2(__builtin_amdgcn_perm(bb, a, 0x05040100u), pw, acc[2 * j]);
          acc[2 * j + 1] = dot2(__builtin_amdgcn_perm(bb, a, 0x07060302u), pw, acc[2 * j + 1]);
        }
      }
      if (m1 > m0) {
#pragma unroll
        for (int j = 0; j < 8; ++j) asm volatile("" : "+v"(acc[j]));
      }
    }
    STAMP(6);
    float a4[4], a2[2];
#pragma unroll
    for (int u = 0; u < 4; ++u) a4[u] = tr32(acc[u], acc[u + 4]);
#pragma unroll
    for (int u = 0; u < 2; ++u) a2[u] = tr16(a4[u], a4[u + 2]);
    const float o1 = trd<ROR8>(a2[0], a2[1], lane & 8);
    const int dim = (lane & 7) * 8 + ((lane >> 5) & 1) * 4 + ((lane >> 4) & 1) * 2 + ((lane >> 3) & 1);
    const float oh = rbf(o1);
    const unsigned hl = pack2(oh, o1 - oh);
    *(LAS u16*)(L + L_OA + sw * 144 + dim * 2) = (u16)hl;
    *(LAS u16*)(L + L_OA + (8 + sw) * 144 + dim * 2) = (u16)(hl >> 16);
    waitlds();
    if (lane == 0) lds_inc(flags + F_O);
  } else {
    const int a = wave;
    const rsrc_t rx = mkrsrc(p.x, 2 * D * 2);
    const int l56 = lane < 56 ? lane : 55, wsmod = WS_MOD + xcd * MOD_REP;
    v4u xa, xb; unsigned cv0, cv1, xd0, xd1;
    const bool qhelp = QSHIFT && !q3 && jn >= 16;
    const bool do_q = a == 0 ? !(QSHIFT && q3) : (a == 3 && qhelp);
    const int qcu = a == 0 ? cu : xcd * 32 + (((jn < 17 ? 7 : jn - 17) * 4 + 3 + CROT) & 31);
    const int qh = qcu >> 4, rq = (qcu & 15) >> 2, cbq = qcu & 3;
    const int l_wq = a == 0 ? L_WQ : L_GU + 16384, l_h1 = a == 0 ? L_H1 : L_H1 + 512;
    const int qk0 = rq < 2 ? rq * 128 : 64 + rq * 96, qnr = rq < 2 ? 128 : 96;
    const int qr = 2 * lane < qnr ? 2 * lane : qnr - 2;
    const int mk0 = mpart < 2 ? mpart * 128 : 64 + mpart * 96, mnr = mpart < 2 ? 128 : 96;
    if (is_mod && a == 2) {
      const rsrc_t rc = mkrsrc(p.c, 2 * D * 2);
      const int mr = 2 * lane < mnr ? 2 * lane : mnr - 2;
      cv0 = __builtin_amdgcn_raw_buffer_load_b32(rc, (mk0 + mr) * 2, 0, 0);
      cv1 = __builtin_amdgcn_raw_buffer_load_b32(rc, (D + mk0 + mr) * 2, 0, 0);
      const GAS unsigned char* src = (const GAS unsigned char*)p.wmod + (size_t)(mk0 + (lane >> 3)) * (6 * D * 2) + mb * 128 + (lane & 7) * 16;
#pragma unroll
      for (int i = 0; i < 16; ++i)
        if (i < 12 || mpart < 2)
          __builtin_amdgcn_global_load_lds((const GAS unsigned*)(src + (size_t)i * 8 * (6 * D * 2)), (LAS unsigned*)(L + L_WM + i * 1024), 16, 0, POL_W);
    }
    if (do_q) {
      xa = __builtin_amdgcn_raw_buffer_load_b128(rx, l56 * 16, 0, 0);
      xb = __builtin_amdgcn_raw_buffer_load_b128(rx, D * 2 + l56 * 16, 0, 0);
      xd0 = __builtin_amdgcn_raw_buffer_load_b32(rx, (qk0 + qr) * 2, 0, 0);
      xd1 = __builtin_amdgcn_raw_buffer_load_b32(rx, (D + qk0 + qr) * 2, 0, 0);
      const GAS unsigned char* src = (const GAS unsigned char*)p.wq + (size_t)(qk0 + (lane >> 3)) * (NH * DH * 2) + (qh * 256 + cbq * 64) * 2 + (lane & 7) * 16;
#pragma unroll
      for (int i = 0; i < 16; ++i)
        if (i < 12 || rq < 2)
          __builtin_amdgcn_global_load_lds((const GAS unsigned*)(src + (size_t)i * 8 * (NH * DH * 2)), (LAS unsigned*)(L + l_wq + i * 1024), 16, 0, POL_W);
    }
    bar();
    if (a == 0) {
      T = __builtin_amdgcn_readfirstlane(e0) + 1u;
      if (lane == 0) { flags[F_T] = T; waitlds(); flags[F_TV] = 1u; }
    } else {
      lds_wait(flags + F_TV, 1u);
      T = __builtin_amdgcn_readfirstlane(flags[F_T]);
    }
    if (!is_mod && a == 2 && lane == 0) lds_inc(flags + F_U);
    if (is_mod && a == 2) {
      {
        const float c00 = lo16(cv0), c01 = hi16(cv0), c10 = lo16(cv1), c11 = hi16(cv1);
        const float s00 = c00 / (1.f + __expf(-c00)), s01 = c01 / (1.f + __expf(-c01));
        const float s10 = c10 / (1.f + __expf(-c10)), s11 = c11 / (1.f + __expf(-c11));
        const bool live = 2 * lane < mnr;
        *(LAS unsigned*)(L + L_H2 + lane * 4) = live ? pack2(s00, s01) : 0u;
        *(LAS unsigned*)(L + L_H2 + 256 + lane * 4) = live ? pack2(s10, s11) : 0u;
      }
      waitvm<0>();
      if (lane == 0) flags[F_MOD] = 1u;
      v4f d[4];
#pragma unroll
      for (int t = 0; t < 4; ++t) d[t] = v4f{0.f, 0.f, 0.f, 0.f};
      LAS unsigned char* mq = L + L_WM + tr_row * 128 + tr_col;
#pragma unroll
      for (int s = 0; s < 4; ++s) {
        const v4u aa = *(const LAS v4u*)(L + L_H2 + (lane & 1) * 256 + s * 64 + g16 * 16);
#pragma unroll
        for (int t = 0; t < 4; ++t) d[t] = mfma(aa, bfrag(mq + (s < 3 || mpart < 2 ? s : 0) * 4096 + t * 32, 128), d[t]);
      }
      waitlds();
      if (lane == 0) lds_inc(flags + F_U);
      float m0 = g16 == 0 ? d[0][0] : (g16 == 1 ? d[1][0] : (g16 == 2 ? d[2][0] : d[3][0]));
      float m1 = g16 == 0 ? d[0][1] : (g16 == 1 ? d[1][1] : (g16 == 2 ? d[2][1] : d[3][1]));
      if (mpart != 0) {
        publish(rs, WS_MP + ((mb * 3 + mpart - 1) * 64 + lane) * 16, __float_as_uint(m0), __float_as_uint(m1), T);
      } else {
        int off[3]; v4u g[3];
#pragma unroll
        for (int k = 0; k < 3; ++k) off[k] = WS_MP + ((mb * 3 + k) * 64 + lane) * 16;
        nap_until(now(), G_MP);
        gather<3, 8>(rs, off, g, T);
        m0 = (m0 + __uint_as_float(g[0].x)) + (__uint_as_float(g[1].x) + __uint_as_float(g[2].x));
        m1 = (m1 + __uint_as_float(g[0].z)) + (__uint_as_float(g[1].z) + __uint_as_float(g[2].z));
        const unsigned gg = pack2(m0, m1), gn = dppu<XOR1>(gg);
        const unsigned lo_ = (lane & 1) ? gn : gg, hi_ = (lane & 1) ? gg : gn;
#pragma unroll
        for (int k = 0; k < NREP / 2; ++k) publish(rs, WS_MOD + (2 * k + (lane & 1)) * MOD_REP + (mb * 32 + (lane >> 1)) * 16, lo_, hi_, T);
        if (lane == 0) flags[F_MOD] = 2u;
      }
      STAMP(1);
    }
    if ((a == 0 || a == 3) && !do_q && lane == 0) lds_inc(flags + F_U);
    if (do_q) {
      waitvm<0>();
      v4u bq[16];
      {
        LAS unsigned char* qq = L + l_wq + tr_row * 128 + tr_col;
#pragma unroll
        for (int s = 0; s < 4; ++s)
#pragma unroll
          for (int t = 0; t < 4; ++t) bq[s * 4 + t] = bfrag(qq + (s < 3 || rq < 2 ? s : 0) * 4096 + t * 32, 128);
      }
      waitlds();
      if (lane == 0) lds_inc(flags + F_U);
      float sm0 = 0.f, sm1 = 0.f;
#pragma unroll
      for (int j = 0; j < 4; ++j) { sm0 += lo16(xa[j]) + hi16(xa[j]); sm1 += lo16(xb[j]) + hi16(xb[j]); }
      if (lane >= 56) { sm0 = 0.f; sm1 = 0.f; }
      const float mean0 = wave_sum(sm0) * (1.f / D), mean1 = wave_sum(sm1) * (1.f / D);
      float v0 = 0.f, v1 = 0.f;
#pragma unroll
      for (int j = 0; j < 4; ++j) {
        float d = lo16(xa[j]) - mean0; v0 = fmaf(d, d, v0); d = hi16(xa[j]) - mean0; v0 = fmaf(d, d, v0);
        d = lo16(xb[j]) - mean1; v1 = fmaf(d, d, v1); d = hi16(xb[j]) - mean1; v1 = fmaf(d, d, v1);
      }
      if (lane >= 56) { v0 = 0.f; v1 = 0.f; }
      const float rs0 = ln_rsd(wave_sum(v0) * (1.f / D)), rs1 = ln_rsd(wave_sum(v1) * (1.f / D));
      const float mb0 = rbf(mean0), mb1 = rbf(mean1);
      const float n00 = rbf(rbf(lo16(xd0) - mb0) * rs0), n01 = rbf(rbf(hi16(xd0) - mb0) * rs0);
      const float n10 = rbf(rbf(lo16(xd1) - mb1) * rs1), n11 = rbf(rbf(hi16(xd1) - mb1) * rs1);
      {
        int off[2] = {wsmod + ((qk0 + qr) >> 1) * 16, wsmod + (224 + ((qk0 + qr) >> 1)) * 16}; v4u g[2];
        nap_until(tstart, G_MOD);
        gather<2, PSLP>(rs, off, g, T);
        STAMP(2);
        const float h00 = rbf(n00 * rbf(1.f + lo16(g[1].x))) + lo16(g[0].x), h10 = rbf(n10 * rbf(1.f + hi16(g[1].x))) + hi16(g[0].x);
        const float h01 = rbf(n01 * rbf(1.f + lo16(g[1].z))) + lo16(g[0].z), h11 = rbf(n11 * rbf(1.f + hi16(g[1].z))) + hi16(g[0].z);
        const bool live = 2 * lane < qnr;
        *(LAS unsigned*)(L + l_h1 + lane * 4) = live ? pack2(h00, h01) : 0u;
        *(LAS unsigned*)(L + l_h1 + 256 + lane * 4) = live ? pack2(h10, h11) : 0u;
      }
      {
        v4f d[4];
#pragma unroll
        for (int t = 0; t < 4; ++t) d[t] = v4f{0.f, 0.f, 0.f, 0.f};
#pragma unroll
        for (int s = 0; s < 4; ++s) {
          const v4u aa = *(const LAS v4u*)(L + l_h1 + (lane & 1) * 256 + s * 64 + g16 * 16);
#pragma unroll
          for (int t = 0; t < 4; ++t) d[t] = mfma(aa, bq[s * 4 + t], d[t]);
        }
        const float q0 = g16 == 0 ? d[0][0] : (g16 == 1 ? d[1][0] : (g16 == 2 ? d[2][0] : d[3][0]));
        const float q1 = g16 == 0 ? d[0][1] : (g16 == 1 ? d[1][1] : (g16 == 2 ? d[2][1] : d[3][1]));
        const bool odd = lane & 1;
        const float rcv = dpp<XOR1>(odd ? q0 : q1);
        publish(rs, WS_Q + ((((lane & 1) * 16 + qh) * 4 + rq) * 128 + cbq * 32 + (lane >> 1)) * 16,
                __float_as_uint(odd ? rcv : q0), __float_as_uint(odd ? q1 : rcv), T);
      }
      STAMP(3);
    }
    if (a == 0) {
      {
        int off[8]; v4u g[8];
#pragma unroll
        for (int k = 0; k < 8; ++k) off[k] = WS_Q + bh * 8192 + (k * 64 + lane) * 16;
        if (do_q) nap_until(now(), G_Q); else nap_until(tstart, G_Q3);
        gather<8, PSLP>(rs, off, g, T);
#pragma unroll
        for (int u = 0; u < 2; ++u) {
          const float qa = (__uint_as_float(g[u].x) + __uint_as_float(g[2 + u].x)) + (__uint_as_float(g[4 + u].x) + __uint_as_float(g[6 + u].x));
          const float qb = (__uint_as_float(g[u].z) + __uint_as_float(g[2 + u].z)) + (__uint_as_float(g[4 + u].z) + __uint_as_float(g[6 + u].z));
          *(LAS unsigned*)(L + L_Q + (u * 64 + lane) * 4) = pack2(qa, qb);
        }
        waitlds();
        if (lane == 0) flags[F_Q] = 1u;
      }
      STAMP(4);
      lds_wait(flags + F_SC, 8u);
      {
        int off[16]; v4u g[16];
#pragma unroll
        for (int k = 0; k < 16; ++k) off[k] = WS_SC + bh * 16384 + (((k ^ (kh * 8)) * 64) + lane) * 16;
        nap_until(now(), G_SC);
        gather<16, PSLP>(rs, off, g, T);
        STAMP(5);
        float m = -INFINITY;
#pragma unroll
        for (int k = 0; k < 16; ++k) m = fmaxf(fmaxf(fmaxf(lo16(g[k].x), hi16(g[k].x)), fmaxf(lo16(g[k].z), hi16(g[k].z))), m);
        m = wave_max(m);
        const float cs = 0.0625f * 1.4426950408889634f;
        const float mc = m * cs;
        float e[32]; float l = 0.f;
#pragma unroll
        for (int k = 0; k < 16; ++k) {
          const float e0_ = __builtin_amdgcn_exp2f(fmaf(lo16(g[k].x), cs, -mc)), e1_ = __builtin_amdgcn_exp2f(fmaf(hi16(g[k].x), cs, -mc));
          const float e2_ = __builtin_amdgcn_exp2f(fmaf(lo16(g[k].z), cs, -mc)), e3_ = __builtin_amdgcn_exp2f(fmaf(hi16(g[k].z), cs, -mc));
          l += (e0_ + e1_) + (e2_ + e3_);
          if (k < 8) { e[4 * k] = e0_; e[4 * k + 1] = e1_; e[4 * k + 2] = e2_; e[4 * k + 3] = e3_; }
        }
        l = wave_sum(l);
        const float rl = rcp_nr(l);
        const int t = lane & 15;
        const int half = t >> 3, j0 = (t >> 2) & 1, j1 = (t >> 1) & 1, j4 = t & 1;
#pragma unroll
        for (int k = 0; k < 8; ++k) {
          const int cq = k >> 1;
          const int wp = (k & 1) * 4 + (lane >> 4);
          const int wc = (j1 << 2) | (j0 << 1) | (wp >> 2), gg = ((wp & 3) << 1) | half, mm = (cq << 2) | (j4 << 1);
          *(LAS v2u*)(L + L_P + ((wc * 8 + gg) * 16 + mm) * 4) = v2u{pack2(e[4 * k] * rl, e[4 * k + 1] * rl), pack2(e[4 * k + 2] * rl, e[4 * k + 3] * rl)};
        }
        waitlds();
        if (lane == 0) flags[F_P] = 1u;
      }
      STAMP(6);
    } else if (a == 1) {
      if (CODEWARM) {
        unsigned long long pc; unsigned len;
        asm volatile("s_getpc_b64 %0\n.Lfb_here:\n\ts_mov_b32 %1, .Lfb_end-.Lfb_here" : "=s"(pc), "=s"(len));
        const unsigned o = (unsigned)((li & 3) * 64 + lane) * 128u;
        const unsigned t_ = *(const GAS unsigned*)((pc & ~3ull) + (o < len ? o : 0u));
        asm volatile("" ::"v"(t_));
      }
      lds_wait(flags + F_KI, 1u);
      const GAS unsigned char* src = (const GAS unsigned char*)p.wo + (size_t)(h * 256 + dq * 64) * (D * 2) + lane * 16;
#pragma unroll
#define NOWO 0
#define POL_WO POL_W
      for (int i = 0; i < (NOWO ? 0 : 56); ++i) {
        if (i >= WDEPTH) waitvm<WDEPTH - 1>();
        __builtin_amdgcn_global_load_lds((const GAS unsigned*)(src + i * 1024), (LAS unsigned*)(L + L_WO + i * 1024), 16, 0, POL_WO);
      }
      waitvm<0>();
      if (lane == 0) flags[F_WO] = 1u;
      STAMP(2);
    }
    if (a == 3 || (DUALP && a == 2)) {
      if (is_red) {
        const unsigned xr = __builtin_amdgcn_raw_buffer_load_b32(rx, (rb * D + rchunk * 8 + (lane & 3) * 2) * 2, 0, 0);
        int off[2] = {wsmod + (D + rchunk * 4 + (lane & 3)) * 16, wsmod + (5 * D / 2 + rchunk * 4 + (lane & 3)) * 16}; v4u g[2];
        lds_wait(flags + F_KI, 1u);
        gather<2, 32>(rs, off, g, T);
        kp[0] = rb ? hi16(g[0].x) : lo16(g[0].x); kp[1] = rb ? hi16(g[0].z) : lo16(g[0].z);
        kp[2] = rb ? hi16(g[1].x) : lo16(g[1].x); kp[3] = rb ? hi16(g[1].z) : lo16(g[1].z);
        kp[4] = lo16(xr); kp[5] = hi16(xr);
        STAMP(2);
      }
    }
    if (is_ffn) {
      const int col2 = ((a & 1) * 56 + l56) * 2;
      int off[4] = {wsmod + (2 * D + col2) * 16, wsmod + (2 * D + col2 + 1) * 16, wsmod + (3 * D / 2 + col2) * 16, wsmod + (3 * D / 2 + col2 + 1) * 16};
      v4u g[4];
      lds_wait(flags + F_KI, 1u);
      gather<4, 32>(rs, off, g, T);
      const bool hb = a >> 1;
      kp[0] = rbf(1.f + (hb ? hi16(g[0].x) : lo16(g[0].x))); kp[1] = rbf(1.f + (hb ? hi16(g[0].z) : lo16(g[0].z)));
      kp[2] = rbf(1.f + (hb ? hi16(g[1].x) : lo16(g[1].x))); kp[3] = rbf(1.f + (hb ? hi16(g[1].z) : lo16(g[1].z)));
      kp[4] = hb ? hi16(g[2].x) : lo16(g[2].x); kp[5] = hb ? hi16(g[2].z) : lo16(g[2].z);
      kp[6] = hb ? hi16(g[3].x) : lo16(g[3].x); kp[7] = hb ? hi16(g[3].z) : lo16(g[3].z);
    }
#pragma unroll
    for (int s = 0; s < 8; ++s) bw[s] = v4u{0u, 0u, 0u, 0u};
    if (a == 0) {
      lds_wait(flags + F_WO, 1u);
#pragma unroll
      for (int s = 0; s < 8; ++s) bw[s] = bfrag(L + L_WO + (32 * (s & 1) + tr_row) * (D * 2) + (24 + (s >> 1)) * 32 + tr_col, D * 2);
    }
    STAMP(7);
  }

  const int n16 = lane & 15;
  const int tsel = g16 == 0 ? t0 : (g16 == 2 ? t1 : t2);
  if (wave == 0 || wave >= 4) {
    lds_spin(flags + F_O, 8u);
    const v4u a0 = *(const LAS v4u*)(L + L_OA + n16 * 144 + g16 * 16), a1 = *(const LAS v4u*)(L + L_OA + n16 * 144 + 64 + g16 * 16);
    const v4f z = {0.f, 0.f, 0.f, 0.f};
    const v4f d0 = mfma(a1, bw[1], mfma(a0, bw[0], z)), d1 = mfma(a1, bw[3], mfma(a0, bw[2], z));
    const v4f d2 = mfma(a1, bw[5], mfma(a0, bw[4], z)), d3 = mfma(a1, bw[7], mfma(a0, bw[6], z));
    const float u1 = tr32((d0[0] + d0[1]) + (d0[2] + d0[3]), (d2[0] + d2[1]) + (d2[2] + d2[3]));
    const float u2 = tr32((d1[0] + d1[1]) + (d1[2] + d1[3]), (d3[0] + d3[1]) + (d3[2] + d3[3]));
    const float v = tr16(u1, u2);
    const float vn = dpp<XOR1>(v);
    const int n = (w0 + g16 * wst) * 16 + n16;
    if ((lane & 1) == 0 && (g16 < 3 || wave == 0))
      publish(rs, WS_P1 + (((b * 56 + (n >> 3)) * 128 + h * 8 + c) * 4 + ((n >> 1) & 3)) * 16, __float_as_uint(v), __float_as_uint(vn), T);
    if (lane == 0) lds_inc(flags + F_PUB);
    STAMP(8);
  }
  if (L2WARM && !is_ffn && wave >= 4 && wave < 8) {
    const int slot = (q3 ? 17 + (li >> 2) : jn) * 4 + wave - 4;
    const int ln = slot * 64 + lane;
    const GAS unsigned char* a_ = ln < 3584 ? (const GAS unsigned char*)((ln & 4) ? p.wu : p.wg) + (size_t)(ln >> 3) * (F * 2) + ((xcd * 448) & ~127) + (ln & 3) * 128
                                            : (const GAS unsigned char*)p.wd + (size_t)xcd * 7 * 32 * (D * 2) + (size_t)(ln < 5152 ? ln - 3584 : 0) * 128;
    const unsigned t_ = *(const GAS unsigned*)a_;
    asm volatile("" ::"v"(t_));
  }
  if (is_red) {
    if (wave == 3 || (DUALP && wave == 2)) {
      float x20, x21;
      lds_wait(flags + F_PUB, 9u);
      if (wave == 2) __builtin_amdgcn_s_sleep(DUALP);
      {
        int off[8]; v4u g[8];
#pragma unroll
        for (int k = 0; k < 8; ++k) off[k] = WS_P1 + (rb * 56 + rchunk) * 8192 + (k * 64 + lane) * 16;
        gather<8, 1>(rs, off, g, T);
        STAMP(9);
        float s0 = 0.f, s1 = 0.f;
#pragma unroll
        for (int k = 0; k < 8; k += 2) { s0 += __uint_as_float(g[k].x) + __uint_as_float(g[k + 1].x); s1 += __uint_as_float(g[k].z) + __uint_as_float(g[k + 1].z); }
        s0 += dpp<ROR4>(s0); s1 += dpp<ROR4>(s1); s0 += dpp<ROR8>(s0); s1 += dpp<ROR8>(s1);
        s0 = sum16(s0); s1 = sum16(s1); s0 = sum32(s0); s1 = sum32(s1);
        x20 = rbf(kp[4] + rbf(kp[0] * rbf(s0))); x21 = rbf(kp[5] + rbf(kp[1] * rbf(s1)));
        const unsigned xp = pack2(x20, x21), xq = dppu<XOR1>(xp);
        float sm = x20 + x21; sm += dpp<XOR1>(sm); sm += dpp<XOR2>(sm);
        const float d0 = x20 - sm * 0.125f, d1 = x21 - sm * 0.125f;
        float m2 = fmaf(d0, d0, d1 * d1); m2 += dpp<XOR1>(m2); m2 += dpp<XOR2>(m2);
        const bool stl = (lane & 3) == 1;
        if ((lane & 3) < 3 && lane < 4 * NREP)
          publish(rs, (lane >> 2) * XS_REP + (stl ? WS_ST + (rb * 56 + rchunk) * 16 : WS_X2 + (rb * 112 + rchunk * 2 + ((lane >> 1) & 1)) * 16),
                  stl ? __float_as_uint(sm) : xp, stl ? __float_as_uint(m2) : xq, T);
        STAMP(10);
      }
      {
        int off[4]; v4u g[4];
#pragma unroll
        for (int k = 0; k < 4; ++k) { int pc = k * 64 + lane; pc = pc < 224 ? pc : pc - 32; off[k] = WS_P2 + (rb * 56 + rchunk) * (NF * 64) + pc * 16; }
        if (wave == 2) __builtin_amdgcn_s_sleep(DUALP);
        gather<4, 1>(rs, off, g, T);
        float s0 = (__uint_as_float(g[0].x) + __uint_as_float(g[1].x)) + (__uint_as_float(g[2].x) + (lane < 32 ? __uint_as_float(g[3].x) : 0.f));
        float s1 = (__uint_as_float(g[0].z) + __uint_as_float(g[1].z)) + (__uint_as_float(g[2].z) + (lane < 32 ? __uint_as_float(g[3].z) : 0.f));
        s0 += dpp<ROR4>(s0); s1 += dpp<ROR4>(s1); s0 += dpp<ROR8>(s0); s1 += dpp<ROR8>(s1);
        s0 = sum16(s0); s1 = sum16(s1); s0 = sum32(s0); s1 = sum32(s1);
        const unsigned ov = pack2(x20 + rbf(kp[2] * rbf(s0)), x21 + rbf(kp[3] * rbf(s1)));
        if (lane < 4) ((GAS unsigned*)p.out)[(rb * D + rchunk * 8) / 2 + lane] = ov;
        if (bid == 0 && lane == 0) __builtin_amdgcn_raw_buffer_store_b32(T, rs, WS_EPOCH, 0, AUX_SC1);
        STAMP(11);
      }
    }
  } else if (is_ffn) {
    {
      if (wave >= 1 && wave < 4) lds_wait(flags + F_PUB, 9u);
      const GAS unsigned char* gsrc = (const GAS unsigned char*)((lane & 4) ? p.wu : p.wg) + (size_t)(lane >> 3) * (F * 2) + fidx * 64 + (lane & 3) * 16;
      const GAS unsigned char* dsrc = (const GAS unsigned char*)p.wd + (size_t)fidx * 32 * (D * 2) + lane * 16;
#pragma unroll
      for (int i = 0; i < 7; ++i) {
        const int u = i * 12 + wave;
        const GAS unsigned char* src = u < 56 ? gsrc + (size_t)u * 8 * (F * 2) : dsrc + (u - 56) * 1024;
        glds16(src, (unsigned)(size_t)(L + L_GU) + u * 1024);
      }
      asm volatile("s_waitcnt vmcnt(2)" ::: "memory");
      if (wave < 4) STAMP(1);
      bar();
    }
    v4u bd[3];
    if (wave < 4) {
      v4u bg[14];
      {
        LAS unsigned char* gq = L + L_GU + tr_row * 128 + (lane & 2) * 32 + wave * 16 + (lane & 1) * 8;
#pragma unroll
        for (int s = 0; s < 14; ++s) bg[s] = bfrag(gq + s * 32 * 128, 128);
      }
      const int l56 = lane < 56 ? lane : 55, xbt = wave >> 1;
      {
        int off[2] = {WS_X2 + xcd * XS_REP + (wave * 56 + l56) * 16, WS_ST + xcd * XS_REP + (xbt * 56 + l56) * 16}; v4u g[2];
        gather<2, 1>(rs, off, g, T);
        STAMP(9);
        const float cm = __uint_as_float(g[1].x) * 0.125f;
        const float piv = __uint_as_float(__builtin_amdgcn_readfirstlane(__float_as_uint(cm)));
        const float dm = lane < 56 ? cm - piv : 0.f;
        float r0 = dm, r1 = lane < 56 ? fmaf(8.f * dm, dm, __uint_as_float(g[1].z)) : 0.f;
        r0 += dpp<XOR1>(r0); r1 += dpp<XOR1>(r1); r0 += dpp<XOR2>(r0); r1 += dpp<XOR2>(r1);
        r0 += dpp<XOR7>(r0); r1 += dpp<XOR7>(r1); r0 += dpp<ROR8>(r0); r1 += dpp<ROR8>(r1);
        r0 = sum16(r0); r1 = sum16(r1); r0 = sum32(r0); r1 = sum32(r1);
        const float dmean = r0 * (1.f / 56.f);
        const float var = fmaxf(fmaf(-dmean, dmean, r1 * (1.f / D)), 0.f);
        const float rsd = ln_rsd(var), mbf = rbf(piv + dmean);
        const v2f m2_ = {mbf, mbf}, r2_ = {rsd, rsd};
        v2f xa_ = unpk(g[0].x) - m2_, xb_ = unpk(g[0].z) - m2_;
        xa_ = unpk(pack2(xa_.x, xa_.y)) * r2_; xb_ = unpk(pack2(xb_.x, xb_.y)) * r2_;
        xa_ = unpk(pack2(xa_.x, xa_.y)) * v2f{kp[0], kp[1]}; xb_ = unpk(pack2(xb_.x, xb_.y)) * v2f{kp[2], kp[3]};
        xa_ = unpk(pack2(xa_.x, xa_.y)) + v2f{kp[4], kp[5]}; xb_ = unpk(pack2(xb_.x, xb_.y)) + v2f{kp[6], kp[7]};
        *(LAS v2u*)(L + L_H2 + (wave * 56 + l56) * 8) = v2u{pack2(xa_.x, xa_.y), pack2(xb_.x, xb_.y)};
      }
      asm volatile("s_waitcnt vmcnt(0)" ::: "memory");
      bar();
      v4f da[4];
#pragma unroll
      for (int u = 0; u < 4; ++u) da[u] = v4f{0.f, 0.f, 0.f, 0.f};
      v4u af[14];
#pragma unroll
      for (int s = 0; s < 14; ++s) af[s] = *(const LAS v4u*)(L + L_H2 + (lane & 1) * (D * 2) + s * 64 + g16 * 16);
      __builtin_amdgcn_sched_barrier(0);
#pragma unroll
      for (int s = 0; s < 14; ++s) da[s & 3] = mfma(af[s], bg[s], da[s & 3]);
      bd[0] = bfrag(L + L_DN + tr_row * (D * 2) + t0 * 32 + tr_col, D * 2);
      bd[1] = bfrag(L + L_DN + tr_row * (D * 2) + t1 * 32 + tr_col, D * 2);
      bd[2] = bfrag(L + L_DN + tr_row * (D * 2) + t2 * 32 + tr_col, D * 2);
      const float g0 = rbf((da[0][0] + da[1][0]) + (da[2][0] + da[3][0])), g1 = rbf((da[0][1] + da[1][1]) + (da[2][1] + da[3][1]));
      const float u0 = dpp<ROR8>(g0), u1 = dpp<ROR8>(g1);
      const float k1 = -2.f * 0.7978845608028654f * 1.4426950408889634f;
      const float e0_ = __builtin_amdgcn_exp2f(k1 * fmaf(0.044715f * g0 * g0, g0, g0)), e1_ = __builtin_amdgcn_exp2f(k1 * fmaf(0.044715f * g1 * g1, g1, g1));
      const float ge0 = rbf(g0 * __builtin_amdgcn_rcpf(1.f + e0_)), ge1 = rbf(g1 * __builtin_amdgcn_rcpf(1.f + e1_));
      const unsigned av = pack2(ge0 * u0, ge1 * u1);
      if (lane < 8) {
        *(LAS u16*)(L + L_ACT + (wave * 8 + lane) * 2) = (u16)av;
        *(LAS u16*)(L + L_ACT + 64 + (wave * 8 + lane) * 2) = (u16)(av >> 16);
      }
      STAMP(10);
    } else {
      asm volatile("s_waitcnt vmcnt(0)" ::: "memory");
      bar();
      bd[0] = bfrag(L + L_DN + tr_row * (D * 2) + t0 * 32 + tr_col, D * 2);
      bd[1] = bfrag(L + L_DN + tr_row * (D * 2) + t1 * 32 + tr_col, D * 2);
      bd[2] = bfrag(L + L_DN + tr_row * (D * 2) + t2 * 32 + tr_col, D * 2);
    }
    bar();
    {
      const v4u aa = *(const LAS v4u*)(L + L_ACT + (lane & 1) * 64 + g16 * 16);
      const v4f z = {0.f, 0.f, 0.f, 0.f};
      const v4f d0 = mfma(aa, bd[0], z), d1 = mfma(aa, bd[1], z), d2 = mfma(aa, bd[2], z);
      const float v0 = g16 == 0 ? d0[0] : (g16 == 2 ? d1[0] : d2[0]), v1 = g16 == 0 ? d0[1] : (g16 == 2 ? d1[1] : d2[1]);
      const bool odd = lane & 1;
      const float rcv = dpp<XOR1>(odd ? v0 : v1);
      const int n = tsel * 16 + n16;
      if ((g16 & 1) == 0 || (g16 == 1 && wave < 4))
        publish(rs, WS_P2 + ((((lane & 1) * 56 + (n >> 3)) * NF + fidx) * 4 + ((n >> 1) & 3)) * 16,
                __float_as_uint(odd ? rcv : v0), __float_as_uint(odd ? v1 : rcv), T);
      STAMP(11);
    }
  }
  STAMP_FLUSH();
  asm volatile(".Lfb_end:");
}
}

extern "C" void kernel_launch(void* const* d_in, const int* in_sizes, int n_in, void* d_out, int out_size, void* d_ws,
                              size_t ws_size, hipStream_t stream) {
  Params p;
  p.x = (const u16*)d_in[0]; p.kc = (const u16*)d_in[1]; p.vc = (const u16*)d_in[2];
  p.wq = (const u16*)d_in[3]; p.wo = (const u16*)d_in[4];
  p.wg = (const u16*)d_in[6]; p.wu = (const u16*)d_in[7]; p.wd = (const u16*)d_in[8];
  p.c = (const u16*)d_in[9]; p.wmod = (const u16*)d_in[10];
  p.out = (u16*)d_out; p.ws = (unsigned char*)d_ws;
  (void)hipFuncSetAttribute((const void*)fused_block, hipFuncAttributeMaxDynamicSharedMemorySize, L_END);
  hipLaunchKernelGGL(fused_block, dim3(256), dim3(NTHR), L_END, stream, p);
}
```

```cpp
#include <hip/hip_runtime.h>
#include <stdint.h>

typedef unsigned short u16;
typedef unsigned v4u __attribute__((ext_vector_type(4)));
typedef unsigned v2u __attribute__((ext_vector_type(2)));
typedef float v4f __attribute__((ext_vector_type(4)));
typedef float v2f __attribute__((ext_vector_type(2)));
typedef short v8s __attribute__((ext_vector_type(8)));
typedef short v4s __attribute__((ext_vector_type(4)));
typedef __bf16 bf16x2 __attribute__((ext_vector_type(2)));
#define GAS __attribute__((address_space(1)))
#define LAS __attribute__((address_space(3)))

#define DBG_STAMP 0

namespace {
constexpr int D = 448, NH = 16, SK = 4096, DH = 256, F = 1792;
constexpr int NTHR = 768;
constexpr int NF = 56;
constexpr int AUX_NT = 2, AUX_SC1 = 16, WO_POL = 0;
constexpr unsigned SPIN_CAP = 1u << 16;
constexpr int KDEPTH = 3, VDEPTH = 3, WDEPTH = 8;
constexpr int VEARLY = 8;
constexpr int PCHK = 27;
constexpr unsigned G_MP = 60, G_MOD = 450, G_Q = 150, G_Q3 = 1000, G_SC = 100;
constexpr int PSLP = 24, SCSLP = 8;

constexpr int WS_EPOCH = 0;
constexpr int NREP = 8;
constexpr int MOD_REP = 21504, XS_REP = 5376;
constexpr int WS_MOD = 256;
constexpr int WS_X2 = WS_MOD + NREP * MOD_REP;
constexpr int WS_ST = WS_X2 + 3584;
constexpr int WS_Q = 262144;
static_assert(WS_X2 + NREP * XS_REP <= WS_Q, "ws map");
constexpr int WS_SC = WS_Q + 32 * 8192;
constexpr int WS_P1 = WS_SC + 32 * 16384;
constexpr int WS_P2 = WS_P1 + 2 * 56 * 8192;
constexpr int WS_MP = WS_P2 + 2 * 56 * NF * 64;
constexpr int WS_END = WS_MP + 42 * 3 * 1024;
constexpr int WS_STAMP = 4 << 20;
static_assert(WS_END <= WS_STAMP, "ws map");

constexpr int L_WO = 0;
constexpr int L_GU = 57344;
constexpr int L_DN = L_GU + 57344;
constexpr int L_Q = L_DN + 28672;
constexpr int L_H1 = L_Q + 512;
constexpr int L_P = L_H1 + 1024;
constexpr int L_OA = L_P + 4096;
constexpr int L_H2 = L_OA + 2304;
constexpr int L_WM = L_GU;
constexpr int L_VE = L_GU;
constexpr int L_WQ = L_DN;
constexpr int L_ACT = L_H2 + 1792;
constexpr int L_FLAG = L_ACT + 128;
constexpr int L_END = L_FLAG + 64;
static_assert(L_END <= 160 * 1024 && L_END > 80 * 1024, "lds");
enum { F_Q = 0, F_P = 1, F_WO = 2, F_SC = 3, F_KI = 5, F_TV = 6, F_U = 7, F_T = 8, F_PUB = 9, F_O = 10 };

struct Params {
  const u16 *x, *kc, *vc, *wq, *wo, *wg, *wu, *wd, *c, *wmod;
  u16* out;
  unsigned char* ws;
};

__device__ __forceinline__ float lo16(unsigned w) { return __uint_as_float(w << 16); }
__device__ __forceinline__ float hi16(unsigned w) { return __uint_as_float(w & 0xffff0000u); }
__device__ __forceinline__ unsigned pack2(float a, float b) {
  bf16x2 v = {(__bf16)a, (__bf16)b};
  return __builtin_bit_cast(unsigned, v);
}
__device__ __forceinline__ v2f unpk(unsigned w) { return v2f{lo16(w), hi16(w)}; }
__device__ __forceinline__ float rbf(float f) { return lo16(pack2(f, f)); }
__device__ __forceinline__ float dot2(unsigned a, unsigned b, float c) {
  return __builtin_amdgcn_fdot2_f32_bf16(__builtin_bit_cast(bf16x2, a), __builtin_bit_cast(bf16x2, b), c, false);
}
__device__ __forceinline__ float rcp_nr(float x) { const float r = __builtin_amdgcn_rcpf(x); return fmaf(fmaf(-x, r, 1.f), r, r); }
template <int N> __device__ __forceinline__ void waitvm() {
  asm volatile("" ::: "memory");
  __builtin_amdgcn_s_waitcnt((N & 15) | ((N >> 4) << 14) | 0x0F70);
  asm volatile("" ::: "memory");
}
__device__ __forceinline__ void waitlds() { asm volatile("s_waitcnt lgkmcnt(0)" ::: "memory"); }
__device__ __forceinline__ void bar() {
  __builtin_amdgcn_sched_barrier(0);
  asm volatile("s_waitcnt lgkmcnt(0)" ::: "memory");
  __builtin_amdgcn_s_barrier();
  asm volatile("" ::: "memory");
  __builtin_amdgcn_sched_barrier(0);
}
template <int CTRL> __device__ __forceinline__ float dpp(float x) {
  return __builtin_bit_cast(float, __builtin_amdgcn_mov_dpp(__builtin_bit_cast(int, x), CTRL, 0xf, 0xf, true));
}
template <int CTRL> __device__ __forceinline__ unsigned dppu(unsigned x) {
  return (unsigned)__builtin_amdgcn_mov_dpp((int)x, CTRL, 0xf, 0xf, true);
}
constexpr int XOR1 = 0xB1, XOR2 = 0x4E, XOR7 = 0x141, ROR8 = 0x128, ROR4 = 0x124;
__device__ __forceinline__ float tr32(float a, float b) {
  auto r = __builtin_amdgcn_permlane32_swap(__float_as_uint(a), __float_as_uint(b), false, false);
  return __uint_as_float(r[0]) + __uint_as_float(r[1]);
}
__device__ __forceinline__ float tr16(float a, float b) {
  auto r = __builtin_amdgcn_permlane16_swap(__float_as_uint(a), __float_as_uint(b), false, false);
  return __uint_as_float(r[0]) + __uint_as_float(r[1]);
}
template <int CTRL> __device__ __forceinline__ float trd(float a, float b, bool bit) {
  const float t1 = a + dpp<CTRL>(a), t2 = b + dpp<CTRL>(b);
  return bit ? t2 : t1;
}
__device__ __forceinline__ float sum32(float a) { return tr32(a, a); }
__device__ __forceinline__ float sum16(float a) { return tr16(a, a); }
__device__ __forceinline__ float wave_sum(float v) {
  v += dpp<XOR1>(v); v += dpp<XOR2>(v); v += dpp<XOR7>(v); v += dpp<ROR8>(v);
  v = sum16(v); v = sum32(v);
  return v;
}
__device__ __forceinline__ float wave_max(float v) {
  v = fmaxf(v, dpp<XOR1>(v)); v = fmaxf(v, dpp<XOR2>(v)); v = fmaxf(v, dpp<XOR7>(v)); v = fmaxf(v, dpp<ROR8>(v));
  auto r = __builtin_amdgcn_permlane16_swap(__float_as_uint(v), __float_as_uint(v), false, false);
  v = fmaxf(__uint_as_float(r[0]), __uint_as_float(r[1]));
  auto t = __builtin_amdgcn_permlane32_swap(__float_as_uint(v), __float_as_uint(v), false, false);
  return fmaxf(__uint_as_float(t[0]), __uint_as_float(t[1]));
}
__device__ __forceinline__ v4f mfma(v4u a, v4u b, v4f c) {
  return __builtin_amdgcn_mfma_f32_16x16x32_bf16(__builtin_bit_cast(v8s, a), __builtin_bit_cast(v8s, b), c, 0, 0, 0);
}
__device__ __forceinline__ v4u bfrag(LAS unsigned char* q, int stride) {
  const v2u a = __builtin_bit_cast(v2u, __builtin_amdgcn_ds_read_tr16_b64_v4i16((LAS v4s*)q));
  const v2u b = __builtin_bit_cast(v2u, __builtin_amdgcn_ds_read_tr16_b64_v4i16((LAS v4s*)(q + 4 * stride)));
  return v4u{a.x, a.y, b.x, b.y};
}

__device__ __forceinline__ void glds16(const GAS unsigned char* gsrc, unsigned lds_dst) {
  unsigned keep;
  asm volatile("s_mov_b32 %0, m0\n\ts_mov_b32 m0, %2\n\ts_nop 0\n\tglobal_load_lds_dwordx4 %1, off\n\ts_mov_b32 m0, %0"
               : "=&s"(keep) : "v"(gsrc), "s"(lds_dst) : "memory");
}
__device__ __forceinline__ void glds16_nt(const GAS unsigned char* gsrc, unsigned lds_dst) {
  unsigned keep;
  asm volatile("s_mov_b32 %0, m0\n\ts_mov_b32 m0, %2\n\ts_nop 0\n\tglobal_load_lds_dwordx4 %1, off nt\n\ts_mov_b32 m0, %0"
               : "=&s"(keep) : "v"(gsrc), "s"(lds_dst) : "memory");
}
template <int N> struct ic { static constexpr int value = N; };
typedef __amdgpu_buffer_rsrc_t rsrc_t;
__device__ __forceinline__ v4u ldstream(rsrc_t r, int voff, int soff) { return __builtin_amdgcn_raw_buffer_load_b128(r, voff, soff, AUX_NT); }
__device__ __forceinline__ rsrc_t mkrsrc(const void* p, int bytes) { return __builtin_amdgcn_make_buffer_rsrc((void*)p, 0, bytes, 0x00020000); }
__device__ __forceinline__ void publish(rsrc_t rs, int off, unsigned a, unsigned b, unsigned T) {
  v4u v = {a, T, b, T};
  __builtin_amdgcn_raw_buffer_store_b128(v, rs, off, 0, AUX_SC1);
}
template <int N, int SLP> __device__ __forceinline__ void gather(rsrc_t rs, const int (&off)[N], v4u (&v)[N], unsigned T) {
#pragma unroll
  for (int k = 0; k < N; ++k) v[k] = __builtin_amdgcn_raw_buffer_load_b128(rs, off[k], 0, AUX_SC1);
  for (unsigned spins = 0; spins < SPIN_CAP; ++spins) {
    bool ok = true;
#pragma unroll
    for (int k = 0; k < N; ++k) ok &= (v[k].y == T) & (v[k].w == T);
    if (__all(ok)) break;
    __builtin_amdgcn_s_sleep(SLP);
    asm volatile("" ::: "memory");
#pragma unroll
    for (int k = 0; k < N; ++k)
      if ((v[k].y != T) | (v[k].w != T)) v[k] = __builtin_amdgcn_raw_buffer_load_b128(rs, off[k], 0, AUX_SC1);
  }
}
__device__ __forceinline__ void lds_wait(volatile LAS unsigned* f, unsigned want) {
  for (unsigned spins = 0; spins < (1u << 22); ++spins) {
    if (__builtin_amdgcn_readfirstlane(*f) >= want) break;
    __builtin_amdgcn_s_sleep(1);
  }
  asm volatile("" ::: "memory");
}
__device__ __forceinline__ void lds_spin(volatile LAS unsigned* f, unsigned want) {
  for (unsigned spins = 0; spins < (1u << 24); ++spins)
    if (__builtin_amdgcn_readfirstlane(*f) >= want) break;
  asm volatile("" ::: "memory");
}
__device__ __forceinline__ unsigned now() { return (unsigned)__builtin_amdgcn_s_memrealtime(); }
__device__ __forceinline__ void nap_until(unsigned t0, unsigned ticks) {
  for (int i = 0; i < 4096 && now() - t0 < ticks; ++i) __builtin_amdgcn_s_sleep(4);
}
__device__ __forceinline__ void lds_inc(volatile LAS unsigned* f) {
  __hip_atomic_fetch_add((LAS unsigned*)f, 1u, __ATOMIC_RELAXED, __HIP_MEMORY_SCOPE_WORKGROUP);
}
__device__ __forceinline__ float ln_rsd(float var) { return rcp_nr(rbf(__builtin_amdgcn_sqrtf(rbf(rbf(var) + 1e-5f)))); }

#define STAMP(i)
#define STAMP_FLUSH()

__global__ void __launch_bounds__(NTHR, 3) fused_block(Params p) {
  extern __shared__ __attribute__((aligned(16))) unsigned char smem[];
  LAS unsigned char* L = (LAS unsigned char*)smem;
  volatile LAS unsigned* flags = (volatile LAS unsigned*)(L + L_FLAG);
  const int bid = blockIdx.x, tid = threadIdx.x, lane = tid & 63;
  const int wave = __builtin_amdgcn_readfirstlane(tid >> 6);
  const int xcd = bid & 7, li = bid >> 3;
  const int cu = xcd * 32 + li;
  const int h = cu >> 4, b = (cu >> 3) & 1, c = cu & 7, kh = c >> 2, dq = c & 3, bh = b * 16 + h;
  const bool q3 = (li & 3) == 3;
  const int jn = (li >> 2) * 3 + (li & 3);
  const int mu = q3 ? 136 + (li >> 2) * 8 + xcd : jn * 8 + xcd;
  const int mb = mu >> 2, mpart = mu & 3;
  const bool is_mod = q3 ? li < 16 : jn < 17, is_ffn = !q3 && jn >= 17, is_red = !q3 && jn < 14;
  const int fidx = xcd * 7 + jn - 17;
  const int rb = jn & 1, rchunk = xcd * 7 + (jn >> 1);
  asm volatile("" ::"s"(p.x), "s"(p.kc), "s"(p.vc), "s"(p.wq), "s"(p.wo), "s"(p.c), "s"(p.wmod), "s"(p.ws));
  const rsrc_t rs = mkrsrc(p.ws, WS_END);
  STAMP(0);
  const unsigned tstart = now();
  unsigned e0 = 0u;
  if (wave == 0) e0 = __builtin_amdgcn_raw_buffer_load_b32(rs, WS_EPOCH, 0, AUX_SC1);
  if (tid < 16) flags[tid] = 0u;
  unsigned T;
  v4u bw[8];
  float kp[8];
  const int t0 = wave < 4 ? 16 + wave : wave - 4, t1 = wave < 4 ? 20 + wave : wave + 4, t2 = wave < 4 ? 24 + wave : 0;
  const int w0 = wave < 4 ? 24 : wave - 4, wst = wave < 4 ? 1 : 8;
  const int tr_row = 8 * (lane >> 4) + ((lane >> 2) & 3), tr_col = (lane & 3) * 8, g16 = lane >> 4;

  if (wave >= 4) {
    const int sw = wave - 4;
    const rsrc_t rk = mkrsrc(p.kc, 64 << 20), rv = mkrsrc(p.vc, 64 << 20);
    const int koff = (bh * SK + c * 512) * 512 + sw * 1024 + lane * 16;
    const int voff = (bh * SK + kh * 2048 + sw * 8 + (lane >> 3)) * 512 + dq * 128 + (lane & 7) * 16;
    v4u kb[32], vb[32];
    const bool bfirst = is_mod && mu < 56;
    if (bfirst) bar();
#pragma unroll
    for (int i = 0; i < 32; ++i) {
      if (i == KDEPTH && !bfirst) bar();
      if (i >= KDEPTH) waitvm<KDEPTH - 1>();
      kb[i] = ldstream(rk, koff, i * 8192);
      __builtin_amdgcn_sched_barrier(0);
    }
    if (sw == 0 && lane == 0) flags[F_KI] = 1u;
    STAMP(1);
    lds_wait(flags + F_U, 3u);
    const GAS unsigned char* vsrc = (const GAS unsigned char*)p.vc + voff;
    const unsigned vdst = (unsigned)(size_t)(L + L_VE) + sw * 1024;
    int ev = 0;
    while (ev < VEARLY && __builtin_amdgcn_readfirstlane(flags[F_Q]) == 0u) {
      waitvm<VDEPTH - 1>();
      glds16_nt(vsrc + ev * 32768, vdst + ev * 8192);
      ++ev;
    }
    lds_wait(flags + F_Q, 1u);
    STAMP(2);
    const v4u qv = *(const LAS v4u*)(L + L_Q + (lane & 31) * 16);
    float s2[16];
#pragma unroll
    for (int m = 0; m < 16; ++m) {
      float a = 0.f, bb = 0.f;
#pragma unroll
      for (int j = 0; j < 4; ++j) { a = dot2(kb[2 * m][j], qv[j], a); bb = dot2(kb[2 * m + 1][j], qv[j], bb); }
      s2[m] = tr16(a, bb);
      if ((m & 3) == 3) {
        __builtin_amdgcn_sched_barrier(0);
        if (ev < VEARLY) { waitvm<VDEPTH - 1>(); glds16_nt(vsrc + ev * 32768, vdst + ev * 8192); ++ev; }
        waitvm<VDEPTH - 1>();
        vb[VEARLY + (m >> 2)] = ldstream(rv, voff, (VEARLY + (m >> 2)) * 32768);
        __builtin_amdgcn_sched_barrier(0);
      }
    }
    float s4[8], s8[4], s16[2];
#pragma unroll
    for (int u = 0; u < 8; ++u) s4[u] = trd<ROR8>(s2[2 * u], s2[2 * u + 1], lane & 8);
#pragma unroll
    for (int u = 0; u < 4; ++u) s8[u] = trd<XOR7>(s4[u], s4[u + 4], lane & 4);
#pragma unroll
    for (int u = 0; u < 2; ++u) s16[u] = trd<XOR2>(s8[u], s8[u + 2], lane & 2);
    const float sc = trd<XOR1>(s16[0], s16[1], lane & 1);
    T = __builtin_amdgcn_readfirstlane(flags[F_T]);
    {
      const unsigned pr = pack2(sc, dpp<XOR1>(sc));
      const unsigned pr2 = dppu<XOR2>(pr);
      if ((lane & 3) == 0) publish(rs, WS_SC + bh * 16384 + ((c * 8 + sw) * 16 + (lane >> 2)) * 16, pr, pr2, T);
      if (lane == 0) lds_inc(flags + F_SC);
    }
    STAMP(3);
    while (ev < VEARLY) { waitvm<VDEPTH - 1>(); glds16_nt(vsrc + ev * 32768, vdst + ev * 8192); ++ev; }
    float acc[8];
#pragma unroll
    for (int j = 0; j < 8; ++j) acc[j] = 0.f;
    auto pv = [&](auto m0_, auto m1_) {
      constexpr int M0 = decltype(m0_)::value, M1 = decltype(m1_)::value;
      v4u pp[4];
#pragma unroll
      for (int u = M0 >> 2; u <= (M1 - 1) >> 2; ++u) pp[u] = *(const LAS v4u*)(L + L_P + (sw * 8 + (lane >> 3)) * 64 + u * 16);
#pragma unroll
      for (int m = M0; m < M1; ++m) {
        const unsigned pw = pp[m >> 2][m & 3];
        const v4u va = 2 * m < VEARLY ? *(const LAS v4u*)(L + L_VE + (2 * m * 8 + sw) * 1024 + lane * 16) : vb[2 * m];
        const v4u vc2 = 2 * m + 1 < VEARLY ? *(const LAS v4u*)(L + L_VE + ((2 * m + 1) * 8 + sw) * 1024 + lane * 16) : vb[2 * m + 1];
#pragma unroll
        for (int j = 0; j < 4; ++j) {
          const unsigned a = va[j], bb = vc2[j];
          acc[2 * j] = dot2(__builtin_amdgcn_perm(bb, a, 0x05040100u), pw, acc[2 * j]);
          acc[2 * j + 1] = dot2(__builtin_amdgcn_perm(bb, a, 0x07060302u), pw, acc[2 * j + 1]);
        }
      }
#pragma unroll
      for (int j = 0; j < 8; ++j) asm volatile("" : "+v"(acc[j]));
    };
    constexpr int PA = (PCHK - VDEPTH + 1) / 2, PB = (31 - VDEPTH + 1) / 2;
    bool early = false;
#pragma unroll
    for (int i = 4 + VEARLY; i < 32; ++i) {
      __builtin_amdgcn_sched_barrier(0);
      waitvm<VDEPTH - 1>();
      vb[i] = ldstream(rv, voff, i * 32768);
      __builtin_amdgcn_sched_barrier(0);
      if (i == PCHK) {
        early = __builtin_amdgcn_readfirstlane(flags[F_P]) != 0u;
        asm volatile("" ::: "memory");
        if (early) pv(ic<0>{}, ic<PA>{});
      }
    }
    STAMP(4);
    if (!early) { lds_wait(flags + F_P, 1u); pv(ic<0>{}, ic<PA>{}); }
    STAMP(5);
    pv(ic<PA>{}, ic<PB>{});
    lds_wait(flags + F_WO, 1u);
#pragma unroll
    for (int s = 0; s < 2; ++s) {
      bw[s] = bfrag(L + L_WO + (32 * s + tr_row) * (D * 2) + w0 * 32 + tr_col, D * 2);
      bw[2 + s] = bfrag(L + L_WO + (32 * s + tr_row) * (D * 2) + (w0 + 8) * 32 + tr_col, D * 2);
      bw[4 + s] = bfrag(L + L_WO + (32 * s + tr_row) * (D * 2) + (w0 + 16) * 32 + tr_col, D * 2);
    }
    pv(ic<PB>{}, ic<16>{});
    STAMP(6);
    float a4[4], a2[2];
#pragma unroll
    for (int u = 0; u < 4; ++u) a4[u] = tr32(acc[u], acc[u + 4]);
#pragma unroll
    for (int u = 0; u < 2; ++u) a2[u] = tr16(a4[u], a4[u + 2]);
    const float o1 = trd<ROR8>(a2[0], a2[1], lane & 8);
    const int dim = (lane & 7) * 8 + ((lane >> 5) & 1) * 4 + ((lane >> 4) & 1) * 2 + ((lane >> 3) & 1);
    const float oh = rbf(o1);
    const unsigned hl = pack2(oh, o1 - oh);
    *(LAS u16*)(L + L_OA + sw * 144 + dim * 2) = (u16)hl;
    *(LAS u16*)(L + L_OA + (8 + sw) * 144 + dim * 2) = (u16)(hl >> 16);
    waitlds();
    if (lane == 0) lds_inc(flags + F_O);
  } else {
    const int a = wave;
#pragma unroll
    for (int j = 0; j < 8; ++j) kp[j] = 0.f;
    const rsrc_t rx = mkrsrc(p.x, 2 * D * 2);
    const int l56 = lane < 56 ? lane : 55, wsmod = WS_MOD + xcd * MOD_REP;
    v4u xa, xb; unsigned cv0, cv1, xd0, xd1;
    const bool qhelp = !q3 && jn >= 16;
    const bool do_q = a == 0 ? !(q3) : (a == 3 && qhelp);
    const int qcu = a == 0 ? cu : xcd * 32 + (jn < 17 ? 7 : jn - 17) * 4 + 3;
    const int qh = qcu >> 4, rq = (qcu & 15) >> 2, cbq = qcu & 3;
    const int l_wq = a == 0 ? L_WQ : L_GU + 16384, l_h1 = a == 0 ? L_H1 : L_H1 + 512;
    const int qk0 = rq < 2 ? rq * 128 : 64 + rq * 96, qnr = rq < 2 ? 128 : 96;
    const int qr = 2 * lane < qnr ? 2 * lane : qnr - 2;
    const int mk0 = mpart < 2 ? mpart * 128 : 64 + mpart * 96, mnr = mpart < 2 ? 128 : 96;
    if (is_mod && a == 2) {
      const rsrc_t rc = mkrsrc(p.c, 2 * D * 2);
      const int mr = 2 * lane < mnr ? 2 * lane : mnr - 2;
      cv0 = __builtin_amdgcn_raw_buffer_load_b32(rc, (mk0 + mr) * 2, 0, 0);
      cv1 = __builtin_amdgcn_raw_buffer_load_b32(rc, (D + mk0 + mr) * 2, 0, 0);
      const GAS unsigned char* src = (const GAS unsigned char*)p.wmod + (size_t)(mk0 + (lane >> 3)) * (6 * D * 2) + mb * 128 + (lane & 7) * 16;
#pragma unroll
      for (int i = 0; i < 16; ++i)
        if (i < 12 || mpart < 2)
          __builtin_amdgcn_global_load_lds((const GAS unsigned*)(src + (size_t)i * 8 * (6 * D * 2)), (LAS unsigned*)(L + L_WM + i * 1024), 16, 0, AUX_NT);
    }
    if (do_q) {
      xa = __builtin_amdgcn_raw_buffer_load_b128(rx, l56 * 16, 0, 0);
      xb = __builtin_amdgcn_raw_buffer_load_b128(rx, D * 2 + l56 * 16, 0, 0);
      xd0 = __builtin_amdgcn_raw_buffer_load_b32(rx, (qk0 + qr) * 2, 0, 0);
      xd1 = __builtin_amdgcn_raw_buffer_load_b32(rx, (D + qk0 + qr) * 2, 0, 0);
      const GAS unsigned char* src = (const GAS unsigned char*)p.wq + (size_t)(qk0 + (lane >> 3)) * (NH * DH * 2) + (qh * 256 + cbq * 64) * 2 + (lane & 7) * 16;
#pragma unroll
      for (int i = 0; i < 16; ++i)
        if (i < 12 || rq < 2)
          __builtin_amdgcn_global_load_lds((const GAS unsigned*)(src + (size_t)i * 8 * (NH * DH * 2)), (LAS unsigned*)(L + l_wq + i * 1024), 16, 0, AUX_NT);
    }
    bar();
    if (a == 0) {
      T = __builtin_amdgcn_readfirstlane(e0) + 1u;
      if (lane == 0) { flags[F_T] = T; waitlds(); flags[F_TV] = 1u; }
    } else {
      lds_wait(flags + F_TV, 1u);
      T = __builtin_amdgcn_readfirstlane(flags[F_T]);
    }
    if (!is_mod && a == 2 && lane == 0) lds_inc(flags + F_U);
    if (is_mod && a == 2) {
      {
        const float c00 = lo16(cv0), c01 = hi16(cv0), c10 = lo16(cv1), c11 = hi16(cv1);
        const float s00 = c00 / (1.f + __expf(-c00)), s01 = c01 / (1.f + __expf(-c01));
        const float s10 = c10 / (1.f + __expf(-c10)), s11 = c11 / (1.f + __expf(-c11));
        const bool live = 2 * lane < mnr;
        *(LAS unsigned*)(L + L_H2 + lane * 4) = live ? pack2(s00, s01) : 0u;
        *(LAS unsigned*)(L + L_H2 + 256 + lane * 4) = live ? pack2(s10, s11) : 0u;
      }
      waitvm<0>();
      v4f d[4];
#pragma unroll
      for (int t = 0; t < 4; ++t) d[t] = v4f{0.f, 0.f, 0.f, 0.f};
      LAS unsigned char* mq = L + L_WM + tr_row * 128 + tr_col;
#pragma unroll
      for (int s = 0; s < 4; ++s) {
        const v4u aa = *(const LAS v4u*)(L + L_H2 + (lane & 1) * 256 + s * 64 + g16 * 16);
#pragma unroll
        for (int t = 0; t < 4; ++t) d[t] = mfma(aa, bfrag(mq + (s < 3 || mpart < 2 ? s : 0) * 4096 + t * 32, 128), d[t]);
      }
      waitlds();
      if (lane == 0) lds_inc(flags + F_U);
      float m0 = g16 == 0 ? d[0][0] : (g16 == 1 ? d[1][0] : (g16 == 2 ? d[2][0] : d[3][0]));
      float m1 = g16 == 0 ? d[0][1] : (g16 == 1 ? d[1][1] : (g16 == 2 ? d[2][1] : d[3][1]));
      if (mpart != 0) {
        publish(rs, WS_MP + ((mb * 3 + mpart - 1) * 64 + lane) * 16, __float_as_uint(m0), __float_as_uint(m1), T);
      } else {
        int off[3]; v4u g[3];
#pragma unroll
        for (int k = 0; k < 3; ++k) off[k] = WS_MP + ((mb * 3 + k) * 64 + lane) * 16;
        nap_until(now(), G_MP);
        gather<3, 8>(rs, off, g, T);
        m0 = (m0 + __uint_as_float(g[0].x)) + (__uint_as_float(g[1].x) + __uint_as_float(g[2].x));
        m1 = (m1 + __uint_as_float(g[0].z)) + (__uint_as_float(g[1].z) + __uint_as_float(g[2].z));
        const unsigned gg = pack2(m0, m1), gn = dppu<XOR1>(gg);
        const unsigned lo_ = (lane & 1) ? gn : gg, hi_ = (lane & 1) ? gg : gn;
#pragma unroll
        for (int k = 0; k < NREP / 2; ++k) publish(rs, WS_MOD + (2 * k + (lane & 1)) * MOD_REP + (mb * 32 + (lane >> 1)) * 16, lo_, hi_, T);
      }
      STAMP(1);
    }
    if ((a == 0 || a == 3) && !do_q && lane == 0) lds_inc(flags + F_U);
    if (do_q) {
      waitvm<0>();
      v4u bq[16];
      {
        LAS unsigned char* qq = L + l_wq + tr_row * 128 + tr_col;
#pragma unroll
        for (int s = 0; s < 4; ++s)
#pragma unroll
          for (int t = 0; t < 4; ++t) bq[s * 4 + t] = bfrag(qq + (s < 3 || rq < 2 ? s : 0) * 4096 + t * 32, 128);
      }
      waitlds();
      if (lane == 0) lds_inc(flags + F_U);
      float sm0 = 0.f, sm1 = 0.f;
#pragma unroll
      for (int j = 0; j < 4; ++j) { sm0 += lo16(xa[j]) + hi16(xa[j]); sm1 += lo16(xb[j]) + hi16(xb[j]); }
      if (lane >= 56) { sm0 = 0.f; sm1 = 0.f; }
      const float mean0 = wave_sum(sm0) * (1.f / D), mean1 = wave_sum(sm1) * (1.f / D);
      float v0 = 0.f, v1 = 0.f;
#pragma unroll
      for (int j = 0; j < 4; ++j) {
        float d = lo16(xa[j]) - mean0; v0 = fmaf(d, d, v0); d = hi16(xa[j]) - mean0; v0 = fmaf(d, d, v0);
        d = lo16(xb[j]) - mean1; v1 = fmaf(d, d, v1); d = hi16(xb[j]) - mean1; v1 = fmaf(d, d, v1);
      }
      if (lane >= 56) { v0 = 0.f; v1 = 0.f; }
      const float rs0 = ln_rsd(wave_sum(v0) * (1.f / D)), rs1 = ln_rsd(wave_sum(v1) * (1.f / D));
      const float mb0 = rbf(mean0), mb1 = rbf(mean1);
      const float n00 = rbf(rbf(lo16(xd0) - mb0) * rs0), n01 = rbf(rbf(hi16(xd0) - mb0) * rs0);
      const float n10 = rbf(rbf(lo16(xd1) - mb1) * rs1), n11 = rbf(rbf(hi16(xd1) - mb1) * rs1);
      {
        int off[2] = {wsmod + ((qk0 + qr) >> 1) * 16, wsmod + (224 + ((qk0 + qr) >> 1)) * 16}; v4u g[2];
        nap_until(tstart, G_MOD);
        gather<2, PSLP>(rs, off, g, T);
        STAMP(2);
        const float h00 = rbf(n00 * rbf(1.f + lo16(g[1].x))) + lo16(g[0].x), h10 = rbf(n10 * rbf(1.f + hi16(g[1].x))) + hi16(g[0].x);
        const float h01 = rbf(n01 * rbf(1.f + lo16(g[1].z))) + lo16(g[0].z), h11 = rbf(n11 * rbf(1.f + hi16(g[1].z))) + hi16(g[0].z);
        const bool live = 2 * lane < qnr;
        *(LAS unsigned*)(L + l_h1 + lane * 4) = live ? pack2(h00, h01) : 0u;
        *(LAS unsigned*)(L + l_h1 + 256 + lane * 4) = live ? pack2(h10, h11) : 0u;
      }
      {
        v4f d[4];
#pragma unroll
        for (int t = 0; t < 4; ++t) d[t] = v4f{0.f, 0.f, 0.f, 0.f};
#pragma unroll
        for (int s = 0; s < 4; ++s) {
          const v4u aa = *(const LAS v4u*)(L + l_h1 + (lane & 1) * 256 + s * 64 + g16 * 16);
#pragma unroll
          for (int t = 0; t < 4; ++t) d[t] = mfma(aa, bq[s * 4 + t], d[t]);
        }
        const float q0 = g16 == 0 ? d[0][0] : (g16 == 1 ? d[1][0] : (g16 == 2 ? d[2][0] : d[3][0]));
        const float q1 = g16 == 0 ? d[0][1] : (g16 == 1 ? d[1][1] : (g16 == 2 ? d[2][1] : d[3][1]));
        const bool odd = lane & 1;
        const float rcv = dpp<XOR1>(odd ? q0 : q1);
        publish(rs, WS_Q + ((((lane & 1) * 16 + qh) * 4 + rq) * 128 + cbq * 32 + (lane >> 1)) * 16,
                __float_as_uint(odd ? rcv : q0), __float_as_uint(odd ? q1 : rcv), T);
      }
      STAMP(3);
    }
    if (a == 0) {
      {
        int off[8]; v4u g[8];
#pragma unroll
        for (int k = 0; k < 8; ++k) off[k] = WS_Q + bh * 8192 + (k * 64 + lane) * 16;
        if (do_q) nap_until(now(), G_Q); else nap_until(tstart, G_Q3);
        gather<8, PSLP>(rs, off, g, T);
#pragma unroll
        for (int u = 0; u < 2; ++u) {
          const float qa = (__uint_as_float(g[u].x) + __uint_as_float(g[2 + u].x)) + (__uint_as_float(g[4 + u].x) + __uint_as_float(g[6 + u].x));
          const float qb = (__uint_as_float(g[u].z) + __uint_as_float(g[2 + u].z)) + (__uint_as_float(g[4 + u].z) + __uint_as_float(g[6 + u].z));
          *(LAS unsigned*)(L + L_Q + (u * 64 + lane) * 4) = pack2(qa, qb);
        }
        waitlds();
        if (lane == 0) flags[F_Q] = 1u;
      }
      STAMP(4);
      lds_wait(flags + F_SC, 8u);
      {
        int off[16]; v4u g[16];
#pragma unroll
        for (int k = 0; k < 16; ++k) off[k] = WS_SC + bh * 16384 + (((k ^ (kh * 8)) * 64) + lane) * 16;
        nap_until(now(), G_SC);
        gather<16, SCSLP>(rs, off, g, T);
        STAMP(5);
        float m = -INFINITY;
#pragma unroll
        for (int k = 0; k < 16; ++k) m = fmaxf(fmaxf(fmaxf(lo16(g[k].x), hi16(g[k].x)), fmaxf(lo16(g[k].z), hi16(g[k].z))), m);
        m = wave_max(m);
        const float cs = 0.0625f * 1.4426950408889634f;
        const float mc = m * cs;
        float e[32]; float l = 0.f;
#pragma unroll
        for (int k = 0; k < 16; ++k) {
          const float e0_ = __builtin_amdgcn_exp2f(fmaf(lo16(g[k].x), cs, -mc)), e1_ = __builtin_amdgcn_exp2f(fmaf(hi16(g[k].x), cs, -mc));
          const float e2_ = __builtin_amdgcn_exp2f(fmaf(lo16(g[k].z), cs, -mc)), e3_ = __builtin_amdgcn_exp2f(fmaf(hi16(g[k].z), cs, -mc));
          l += (e0_ + e1_) + (e2_ + e3_);
          if (k < 8) { e[4 * k] = e0_; e[4 * k + 1] = e1_; e[4 * k + 2] = e2_; e[4 * k + 3] = e3_; }
        }
        l = wave_sum(l);
        const float rl = rcp_nr(l);
        const int t = lane & 15;
        const int half = t >> 3, j0 = (t >> 2) & 1, j1 = (t >> 1) & 1, j4 = t & 1;
#pragma unroll
        for (int k = 0; k < 8; ++k) {
          const int cq = k >> 1;
          const int wp = (k & 1) * 4 + (lane >> 4);
          const int wc = (j1 << 2) | (j0 << 1) | (wp >> 2), gg = ((wp & 3) << 1) | half, mm = (cq << 2) | (j4 << 1);
          *(LAS v2u*)(L + L_P + ((wc * 8 + gg) * 16 + mm) * 4) = v2u{pack2(e[4 * k] * rl, e[4 * k + 1] * rl), pack2(e[4 * k + 2] * rl, e[4 * k + 3] * rl)};
        }
        waitlds();
        if (lane == 0) flags[F_P] = 1u;
      }
      STAMP(6);
    } else if (a == 1) {
      {
        unsigned long long pc; int len;
        asm volatile("s_getpc_b64 %0\n.Lfb_here:\n\ts_mov_b32 %1, .Lfb_end-.Lfb_here" : "=s"(pc), "=s"(len));
        const int o = ((li & 3) * 64 + lane) * 128;
        const unsigned t_ = *(const GAS unsigned*)((pc & ~3ull) + (unsigned)(o < len ? o : 0));
        asm volatile("" ::"v"(t_));
      }
      lds_wait(flags + F_KI, 1u);
      const GAS unsigned char* src = (const GAS unsigned char*)p.wo + (size_t)(h * 256 + dq * 64) * (D * 2) + lane * 16;
#pragma unroll
      for (int i = 0; i < 56; ++i) {
        if (i >= WDEPTH) waitvm<WDEPTH - 1>();
        __builtin_amdgcn_global_load_lds((const GAS unsigned*)(src + i * 1024), (LAS unsigned*)(L + L_WO + i * 1024), 16, 0, WO_POL);
      }
      waitvm<0>();
      if (lane == 0) flags[F_WO] = 1u;
      STAMP(2);
    }
    if (a == 3) {
      if (is_red) {
        const unsigned xr = __builtin_amdgcn_raw_buffer_load_b32(rx, (rb * D + rchunk * 8 + (lane & 3) * 2) * 2, 0, 0);
        int off[2] = {wsmod + (D + rchunk * 4 + (lane & 3)) * 16, wsmod + (5 * D / 2 + rchunk * 4 + (lane & 3)) * 16}; v4u g[2];
        lds_wait(flags + F_KI, 1u);
        gather<2, 32>(rs, off, g, T);
        kp[0] = rb ? hi16(g[0].x) : lo16(g[0].x); kp[1] = rb ? hi16(g[0].z) : lo16(g[0].z);
        kp[2] = rb ? hi16(g[1].x) : lo16(g[1].x); kp[3] = rb ? hi16(g[1].z) : lo16(g[1].z);
        kp[4] = lo16(xr); kp[5] = hi16(xr);
        STAMP(2);
      }
    }
    if (is_ffn) {
      const int col2 = ((a & 1) * 56 + l56) * 2;
      int off[4] = {wsmod + (2 * D + col2) * 16, wsmod + (2 * D + col2 + 1) * 16, wsmod + (3 * D / 2 + col2) * 16, wsmod + (3 * D / 2 + col2 + 1) * 16};
      v4u g[4];
      lds_wait(flags + F_KI, 1u);
      gather<4, 32>(rs, off, g, T);
      const bool hb = a >> 1;
      kp[0] = rbf(1.f + (hb ? hi16(g[0].x) : lo16(g[0].x))); kp[1] = rbf(1.f + (hb ? hi16(g[0].z) : lo16(g[0].z)));
      kp[2] = rbf(1.f + (hb ? hi16(g[1].x) : lo16(g[1].x))); kp[3] = rbf(1.f + (hb ? hi16(g[1].z) : lo16(g[1].z)));
      kp[4] = hb ? hi16(g[2].x) : lo16(g[2].x); kp[5] = hb ? hi16(g[2].z) : lo16(g[2].z);
      kp[6] = hb ? hi16(g[3].x) : lo16(g[3].x); kp[7] = hb ? hi16(g[3].z) : lo16(g[3].z);
    }
    if (a == 0) {
      lds_wait(flags + F_WO, 1u);
#pragma unroll
      for (int s = 0; s < 8; ++s) bw[s] = bfrag(L + L_WO + (32 * (s & 1) + tr_row) * (D * 2) + (24 + (s >> 1)) * 32 + tr_col, D * 2);
    }
    STAMP(7);
  }

  const int n16 = lane & 15;
  const int tsel = g16 == 0 ? t0 : (g16 == 2 ? t1 : t2);
  if (wave == 0 || wave >= 4) {
    const int n = (w0 + g16 * wst) * 16 + n16;
    int poff = WS_P1 + (((b * 56 + (n >> 3)) * 128 + h * 8 + c) * 4 + ((n >> 1) & 3)) * 16;
    asm volatile("" : "+v"(poff));
    lds_spin(flags + F_O, 8u);
    const v4u a0 = *(const LAS v4u*)(L + L_OA + n16 * 144 + g16 * 16), a1 = *(const LAS v4u*)(L + L_OA + n16 * 144 + 64 + g16 * 16);
    const v4f z = {0.f, 0.f, 0.f, 0.f};
    const v4f d0 = mfma(a1, bw[1], mfma(a0, bw[0], z)), d1 = mfma(a1, bw[3], mfma(a0, bw[2], z));
    const v4f d2 = mfma(a1, bw[5], mfma(a0, bw[4], z));
    v4f d3 = z;
    if (wave == 0) d3 = mfma(a1, bw[7], mfma(a0, bw[6], z));
    const float u1 = tr32((d0[0] + d0[1]) + (d0[2] + d0[3]), (d2[0] + d2[1]) + (d2[2] + d2[3]));
    const float u2 = tr32((d1[0] + d1[1]) + (d1[2] + d1[3]), (d3[0] + d3[1]) + (d3[2] + d3[3]));
    const float v = tr16(u1, u2);
    const float vn = dpp<XOR1>(v);
    if ((lane & 1) == 0 && (g16 < 3 || wave == 0)) publish(rs, poff, __float_as_uint(v), __float_as_uint(vn), T);
    if (lane == 0) lds_inc(flags + F_PUB);
    STAMP(8);
  }
  if (!is_ffn && wave >= 4 && wave < 8) {
    const int slot = (q3 ? 17 + (li >> 2) : jn) * 4 + wave - 4;
    const int ln = slot * 64 + lane;
    const GAS unsigned char* a_ = ln < 3584 ? (const GAS unsigned char*)((ln & 4) ? p.wu : p.wg) + (size_t)(ln >> 3) * (F * 2) + ((xcd * 448) & ~127) + (ln & 3) * 128
                                            : (const GAS unsigned char*)p.wd + (size_t)xcd * 7 * 32 * (D * 2) + (size_t)(ln < 5152 ? ln - 3584 : 0) * 128;
    const unsigned t_ = *(const GAS unsigned*)a_;
    asm volatile("" ::"v"(t_));
  }
  if (is_red) {
    if (wave == 3) {
      float x20, x21;
      lds_wait(flags + F_PUB, 9u);
      {
        int off[8]; v4u g[8];
#pragma unroll
        for (int k = 0; k < 8; ++k) off[k] = WS_P1 + (rb * 56 + rchunk) * 8192 + (k * 64 + lane) * 16;
        gather<8, 1>(rs, off, g, T);
        STAMP(9);
        float s0 = 0.f, s1 = 0.f;
#pragma unroll
        for (int k = 0; k < 8; k += 2) { s0 += __uint_as_float(g[k].x) + __uint_as_float(g[k + 1].x); s1 += __uint_as_float(g[k].z) + __uint_as_float(g[k + 1].z); }
        s0 += dpp<ROR4>(s0); s1 += dpp<ROR4>(s1); s0 += dpp<ROR8>(s0); s1 += dpp<ROR8>(s1);
        s0 = sum16(s0); s1 = sum16(s1); s0 = sum32(s0); s1 = sum32(s1);
        x20 = rbf(kp[4] + rbf(kp[0] * rbf(s0))); x21 = rbf(kp[5] + rbf(kp[1] * rbf(s1)));
        const unsigned xp = pack2(x20, x21), xq = dppu<XOR1>(xp);
        float sm = x20 + x21; sm += dpp<XOR1>(sm); sm += dpp<XOR2>(sm);
        const float d0 = x20 - sm * 0.125f, d1 = x21 - sm * 0.125f;
        float m2 = fmaf(d0, d0, d1 * d1); m2 += dpp<XOR1>(m2); m2 += dpp<XOR2>(m2);
        const bool stl = (lane & 3) == 1;
        if ((lane & 3) < 3 && lane < 4 * NREP)
          publish(rs, (lane >> 2) * XS_REP + (stl ? WS_ST + (rb * 56 + rchunk) * 16 : WS_X2 + (rb * 112 + rchunk * 2 + ((lane >> 1) & 1)) * 16),
                  stl ? __float_as_uint(sm) : xp, stl ? __float_as_uint(m2) : xq, T);
        STAMP(10);
      }
      {
        int off[4]; v4u g[4];
#pragma unroll
        for (int k = 0; k < 4; ++k) { int pc = k * 64 + lane; pc = pc < 224 ? pc : pc - 32; off[k] = WS_P2 + (rb * 56 + rchunk) * (NF * 64) + pc * 16; }
          gather<4, 1>(rs, off, g, T);
        float s0 = (__uint_as_float(g[0].x) + __uint_as_float(g[1].x)) + (__uint_as_float(g[2].x) + (lane < 32 ? __uint_as_float(g[3].x) : 0.f));
        float s1 = (__uint_as_float(g[0].z) + __uint_as_float(g[1].z)) + (__uint_as_float(g[2].z) + (lane < 32 ? __uint_as_float(g[3].z) : 0.f));
        s0 += dpp<ROR4>(s0); s1 += dpp<ROR4>(s1); s0 += dpp<ROR8>(s0); s1 += dpp<ROR8>(s1);
        s0 = sum16(s0); s1 = sum16(s1); s0 = sum32(s0); s1 = sum32(s1);
        const unsigned ov = pack2(x20 + rbf(kp[2] * rbf(s0)), x21 + rbf(kp[3] * rbf(s1)));
        if (lane < 4) ((GAS unsigned*)p.out)[(rb * D + rchunk * 8) / 2 + lane] = ov;
        if (bid == 0 && lane == 0) __builtin_amdgcn_raw_buffer_store_b32(T, rs, WS_EPOCH, 0, AUX_SC1);
        STAMP(11);
      }
    }
  } else if (is_ffn) {
    {
      if (wave >= 1 && wave < 4) lds_wait(flags + F_PUB, 9u);
      const GAS unsigned char* gsrc = (const GAS unsigned char*)((lane & 4) ? p.wu : p.wg) + (size_t)(lane >> 3) * (F * 2) + fidx * 64 + (lane & 3) * 16;
      const GAS unsigned char* dsrc = (const GAS unsigned char*)p.wd + (size_t)fidx * 32 * (D * 2) + lane * 16;
#pragma unroll
      for (int i = 0; i < 7; ++i) {
        const int u = i * 12 + wave;
        const GAS unsigned char* src = u < 56 ? gsrc + (size_t)u * 8 * (F * 2) : dsrc + (u - 56) * 1024;
        glds16(src, (unsigned)(size_t)(L + L_GU) + u * 1024);
      }
      asm volatile("s_waitcnt vmcnt(2)" ::: "memory");
      if (wave < 4) STAMP(1);
      bar();
    }
    v4u bd[3];
    if (wave < 4) {
      v4u bg[14];
      {
        LAS unsigned char* gq = L + L_GU + tr_row * 128 + (lane & 2) * 32 + wave * 16 + (lane & 1) * 8;
#pragma unroll
        for (int s = 0; s < 14; ++s) bg[s] = bfrag(gq + s * 32 * 128, 128);
      }
      const int l56 = lane < 56 ? lane : 55, xbt = wave >> 1;
      {
        int off[2] = {WS_X2 + xcd * XS_REP + (wave * 56 + l56) * 16, WS_ST + xcd * XS_REP + (xbt * 56 + l56) * 16}; v4u g[2];
        gather<2, 1>(rs, off, g, T);
        STAMP(9);
        const float cm = __uint_as_float(g[1].x) * 0.125f;
        const float piv = __uint_as_float(__builtin_amdgcn_readfirstlane(__float_as_uint(cm)));
        const float dm = lane < 56 ? cm - piv : 0.f;
        float r0 = dm, r1 = lane < 56 ? fmaf(8.f * dm, dm, __uint_as_float(g[1].z)) : 0.f;
        r0 += dpp<XOR1>(r0); r1 += dpp<XOR1>(r1); r0 += dpp<XOR2>(r0); r1 += dpp<XOR2>(r1);
        r0 += dpp<XOR7>(r0); r1 += dpp<XOR7>(r1); r0 += dpp<ROR8>(r0); r1 += dpp<ROR8>(r1);
        r0 = sum16(r0); r1 = sum16(r1); r0 = sum32(r0); r1 = sum32(r1);
        const float dmean = r0 * (1.f / 56.f);
        const float var = fmaxf(fmaf(-dmean, dmean, r1 * (1.f / D)), 0.f);
        const float rsd = ln_rsd(var), mbf = rbf(piv + dmean);
        const v2f m2_ = {mbf, mbf}, r2_ = {rsd, rsd};
        v2f xa_ = unpk(g[0].x) - m2_, xb_ = unpk(g[0].z) - m2_;
        xa_ = unpk(pack2(xa_.x, xa_.y)) * r2_; xb_ = unpk(pack2(xb_.x, xb_.y)) * r2_;
        xa_ = unpk(pack2(xa_.x, xa_.y)) * v2f{kp[0], kp[1]}; xb_ = unpk(pack2(xb_.x, xb_.y)) * v2f{kp[2], kp[3]};
        xa_ = unpk(pack2(xa_.x, xa_.y)) + v2f{kp[4], kp[5]}; xb_ = unpk(pack2(xb_.x, xb_.y)) + v2f{kp[6], kp[7]};
        *(LAS v2u*)(L + L_H2 + (wave * 56 + l56) * 8) = v2u{pack2(xa_.x, xa_.y), pack2(xb_.x, xb_.y)};
      }
      asm volatile("s_waitcnt vmcnt(0)" ::: "memory");
      bar();
      v4f da[4];
#pragma unroll
      for (int u = 0; u < 4; ++u) da[u] = v4f{0.f, 0.f, 0.f, 0.f};
      v4u af[14];
#pragma unroll
      for (int s = 0; s < 14; ++s) af[s] = *(const LAS v4u*)(L + L_H2 + (lane & 1) * (D * 2) + s * 64 + g16 * 16);
      __builtin_amdgcn_sched_barrier(0);
#pragma unroll
      for (int s = 0; s < 14; ++s) da[s & 3] = mfma(af[s], bg[s], da[s & 3]);
      bd[0] = bfrag(L + L_DN + tr_row * (D * 2) + t0 * 32 + tr_col, D * 2);
      bd[1] = bfrag(L + L_DN + tr_row * (D * 2) + t1 * 32 + tr_col, D * 2);
      bd[2] = bfrag(L + L_DN + tr_row * (D * 2) + t2 * 32 + tr_col, D * 2);
      const float g0 = rbf((da[0][0] + da[1][0]) + (da[2][0] + da[3][0])), g1 = rbf((da[0][1] + da[1][1]) + (da[2][1] + da[3][1]));
      const float u0 = dpp<ROR8>(g0), u1 = dpp<ROR8>(g1);
      const float k1 = -2.f * 0.7978845608028654f * 1.4426950408889634f;
      const float e0_ = __builtin_amdgcn_exp2f(k1 * fmaf(0.044715f * g0 * g0, g0, g0)), e1_ = __builtin_amdgcn_exp2f(k1 * fmaf(0.044715f * g1 * g1, g1, g1));
      const float ge0 = rbf(g0 * __builtin_amdgcn_rcpf(1.f + e0_)), ge1 = rbf(g1 * __builtin_amdgcn_rcpf(1.f + e1_));
      const unsigned av = pack2(ge0 * u0, ge1 * u1);
      if (lane < 8) {
        *(LAS u16*)(L + L_ACT + (wave * 8 + lane) * 2) = (u16)av;
        *(LAS u16*)(L + L_ACT + 64 + (wave * 8 + lane) * 2) = (u16)(av >> 16);
      }
      STAMP(10);
    } else {
      asm volatile("s_waitcnt vmcnt(0)" ::: "memory");
      bar();
      bd[0] = bfrag(L + L_DN + tr_row * (D * 2) + t0 * 32 + tr_col, D * 2);
      bd[1] = bfrag(L + L_DN + tr_row * (D * 2) + t1 * 32 + tr_col, D * 2);
      bd[2] = bfrag(L + L_DN + tr_row * (D * 2) + t2 * 32 + tr_col, D * 2);
    }
    bar();
    {
      const v4u aa = *(const LAS v4u*)(L + L_ACT + (lane & 1) * 64 + g16 * 16);
      const v4f z = {0.f, 0.f, 0.f, 0.f};
      const v4f d0 = mfma(aa, bd[0], z), d1 = mfma(aa, bd[1], z), d2 = mfma(aa, bd[2], z);
      const float v0 = g16 == 0 ? d0[0] : (g16 == 2 ? d1[0] : d2[0]), v1 = g16 == 0 ? d0[1] : (g16 == 2 ? d1[1] : d2[1]);
      const bool odd = lane & 1;
      const float rcv = dpp<XOR1>(odd ? v0 : v1);
      const int n = tsel * 16 + n16;
      if ((g16 & 1) == 0 || (g16 == 1 && wave < 4))
        publish(rs, WS_P2 + ((((lane & 1) * 56 + (n >> 3)) * NF + fidx) * 4 + ((n >> 1) & 3)) * 16,
                __float_as_uint(odd ? rcv : v0), __float_as_uint(odd ? v1 : rcv), T);
      STAMP(11);
    }
  }
  STAMP_FLUSH();
  asm volatile(".Lfb_end:");
}
}

extern "C" void kernel_launch(void* const* d_in, const int* in_sizes, int n_in, void* d_out, int out_size, void* d_ws,
                              size_t ws_size, hipStream_t stream) {
  Params p;
  p.x = (const u16*)d_in[0]; p.kc = (const u16*)d_in[1]; p.vc = (const u16*)d_in[2];
  p.wq = (const u16*)d_in[3]; p.wo = (const u16*)d_in[4];
  p.wg = (const u16*)d_in[6]; p.wu = (const u16*)d_in[7]; p.wd = (const u16*)d_in[8];
  p.c = (const u16*)d_in[9]; p.wmod = (const u16*)d_in[10];
  p.out = (u16*)d_out; p.ws = (unsigned char*)d_ws;
  (void)hipFuncSetAttribute((const void*)fused_block, hipFuncAttributeMaxDynamicSharedMemorySize, L_END);
  hipLaunchKernelGGL(fused_block, dim3(256), dim3(NTHR), L_END, stream, p);
}
```

```cpp
#include <hip/hip_runtime.h>
#include <stdint.h>

typedef unsigned short u16;
typedef unsigned v4u __attribute__((ext_vector_type(4)));
typedef unsigned v2u __attribute__((ext_vector_type(2)));
typedef float v4f __attribute__((ext_vector_type(4)));
typedef float v2f __attribute__((ext_vector_type(2)));
typedef short v8s __attribute__((ext_vector_type(8)));
typedef short v4s __attribute__((ext_vector_type(4)));
typedef __bf16 bf16x2 __attribute__((ext_vector_type(2)));
#define GAS __attribute__((address_space(1)))
#define LAS __attribute__((address_space(3)))

namespace {
constexpr int D = 448, NH = 16, SK = 4096, DH = 256, F = 1792;
constexpr int NTHR = 768;
constexpr int NF = 56;
constexpr int AUX_NT = 2, AUX_SC1 = 16, WO_POL = 0;
constexpr unsigned SPIN_CAP = 1u << 16;
constexpr int KDEPTH = 3, VDEPTH = 3, WDEPTH = 8;
constexpr int VEARLY = 8;
constexpr int PCHK = 27;
constexpr unsigned G_MP = 60, G_MOD = 450, G_Q = 150, G_Q3 = 1000, G_SC = 100;
constexpr int PSLP = 24, SCSLP = 8;

constexpr int WS_EPOCH = 0;
constexpr int NREP = 8;
constexpr int MOD_REP = 21504, XS_REP = 5376;
constexpr int WS_MOD = 256;
constexpr int WS_X2 = WS_MOD + NREP * MOD_REP;
constexpr int WS_ST = WS_X2 + 3584;
constexpr int WS_Q = 262144;
static_assert(WS_X2 + NREP * XS_REP <= WS_Q, "ws map");
constexpr int WS_SC = WS_Q + 32 * 8192;
constexpr int WS_P1 = WS_SC + 32 * 16384;
constexpr int WS_P2 = WS_P1 + 2 * 56 * 8192;
constexpr int WS_MP = WS_P2 + 2 * 56 * NF * 64;
constexpr int WS_END = WS_MP + 42 * 3 * 1024;

constexpr int L_WO = 0;
constexpr int L_GU = 57344;
constexpr int L_DN = L_GU + 57344;
constexpr int L_Q = L_DN + 28672;
constexpr int L_H1 = L_Q + 512;
constexpr int L_P = L_H1 + 1024;
constexpr int L_OA = L_P + 4096;
constexpr int L_H2 = L_OA + 2304;
constexpr int L_WM = L_GU;
constexpr int L_VE = L_GU;
constexpr int L_WQ = L_DN;
constexpr int L_ACT = L_H2 + 1792;
constexpr int L_FLAG = L_ACT + 128;
constexpr int L_END = L_FLAG + 64;
static_assert(L_END <= 160 * 1024 && L_END > 80 * 1024, "lds");
enum { F_Q = 0, F_P = 1, F_WO = 2, F_SC = 3, F_KI = 5, F_TV = 6, F_U = 7, F_T = 8, F_PUB = 9, F_O = 10 };

struct Params {
  const u16 *x, *kc, *vc, *wq, *wo, *wg, *wu, *wd, *c, *wmod;
  u16* out;
  unsigned char* ws;
};

__device__ __forceinline__ float lo16(unsigned w) { return __uint_as_float(w << 16); }
__device__ __forceinline__ float hi16(unsigned w) { return __uint_as_float(w & 0xffff0000u); }
__device__ __forceinline__ unsigned pack2(float a, float b) {
  bf16x2 v = {(__bf16)a, (__bf16)b};
  return __builtin_bit_cast(unsigned, v);
}
__device__ __forceinline__ v2f unpk(unsigned w) { return v2f{lo16(w), hi16(w)}; }
__device__ __forceinline__ float rbf(float f) { return lo16(pack2(f, f)); }
__device__ __forceinline__ float dot2(unsigned a, unsigned b, float c) {
  return __builtin_amdgcn_fdot2_f32_bf16(__builtin_bit_cast(bf16x2, a), __builtin_bit_cast(bf16x2, b), c, false);
}
__device__ __forceinline__ float rcp_nr(float x) { const float r = __builtin_amdgcn_rcpf(x); return fmaf(fmaf(-x, r, 1.f), r, r); }
template <int N> __device__ __forceinline__ void waitvm() {
  asm volatile("" ::: "memory");
  __builtin_amdgcn_s_waitcnt((N & 15) | ((N >> 4) << 14) | 0x0F70);
  asm volatile("" ::: "memory");
}
__device__ __forceinline__ void waitlds() { asm volatile("s_waitcnt lgkmcnt(0)" ::: "memory"); }
__device__ __forceinline__ void bar() {
  __builtin_amdgcn_sched_barrier(0);
  asm volatile("s_waitcnt lgkmcnt(0)" ::: "memory");
  __builtin_amdgcn_s_barrier();
  asm volatile("" ::: "memory");
  __builtin_amdgcn_sched_barrier(0);
}
template <int CTRL> __device__ __forceinline__ float dpp(float x) {
  return __builtin_bit_cast(float, __builtin_amdgcn_mov_dpp(__builtin_bit_cast(int, x), CTRL, 0xf, 0xf, true));
}
template <int CTRL> __device__ __forceinline__ unsigned dppu(unsigned x) {
  return (unsigned)__builtin_amdgcn_mov_dpp((int)x, CTRL, 0xf, 0xf, true);
}
constexpr int XOR1 = 0xB1, XOR2 = 0x4E, XOR7 = 0x141, ROR8 = 0x128, ROR4 = 0x124;
__device__ __forceinline__ float tr32(float a, float b) {
  auto r = __builtin_amdgcn_permlane32_swap(__float_as_uint(a), __float_as_uint(b), false, false);
  return __uint_as_float(r[0]) + __uint_as_float(r[1]);
}
__device__ __forceinline__ float tr16(float a, float b) {
  auto r = __builtin_amdgcn_permlane16_swap(__float_as_uint(a), __float_as_uint(b), false, false);
  return __uint_as_float(r[0]) + __uint_as_float(r[1]);
}
template <int CTRL> __device__ __forceinline__ float trd(float a, float b, bool bit) {
  const float t1 = a + dpp<CTRL>(a), t2 = b + dpp<CTRL>(b);
  return bit ? t2 : t1;
}
__device__ __forceinline__ float sum32(float a) { return tr32(a, a); }
__device__ __forceinline__ float sum16(float a) { return tr16(a, a); }
__device__ __forceinline__ float wave_sum(float v) {
  v += dpp<XOR1>(v); v += dpp<XOR2>(v); v += dpp<XOR7>(v); v += dpp<ROR8>(v);
  v = sum16(v); v = sum32(v);
  return v;
}
__device__ __forceinline__ float wave_max(float v) {
  v = fmaxf(v, dpp<XOR1>(v)); v = fmaxf(v, dpp<XOR2>(v)); v = fmaxf(v, dpp<XOR7>(v)); v = fmaxf(v, dpp<ROR8>(v));
  auto r = __builtin_amdgcn_permlane16_swap(__float_as_uint(v), __float_as_uint(v), false, false);
  v = fmaxf(__uint_as_float(r[0]), __uint_as_float(r[1]));
  auto t = __builtin_amdgcn_permlane32_swap(__float_as_uint(v), __float_as_uint(v), false, false);
  return fmaxf(__uint_as_float(t[0]), __uint_as_float(t[1]));
}
__device__ __forceinline__ v4f mfma(v4u a, v4u b, v4f c) {
  return __builtin_amdgcn_mfma_f32_16x16x32_bf16(__builtin_bit_cast(v8s, a), __builtin_bit_cast(v8s, b), c, 0, 0, 0);
}
__device__ __forceinline__ v4u bfrag(LAS unsigned char* q, int stride) {
  const v2u a = __builtin_bit_cast(v2u, __builtin_amdgcn_ds_read_tr16_b64_v4i16((LAS v4s*)q));
  const v2u b = __builtin_bit_cast(v2u, __builtin_amdgcn_ds_read_tr16_b64_v4i16((LAS v4s*)(q + 4 * stride)));
  return v4u{a.x, a.y, b.x, b.y};
}

__device__ __forceinline__ void glds16(const GAS unsigned char* gsrc, unsigned lds_dst) {
  unsigned keep;
  asm volatile("s_mov_b32 %0, m0\n\ts_mov_b32 m0, %2\n\ts_nop 0\n\tglobal_load_lds_dwordx4 %1, off\n\ts_mov_b32 m0, %0"
               : "=&s"(keep) : "v"(gsrc), "s"(lds_dst) : "memory");
}
__device__ __forceinline__ void glds16_nt(const GAS unsigned char* gsrc, unsigned lds_dst) {
  unsigned keep;
  asm volatile("s_mov_b32 %0, m0\n\ts_mov_b32 m0, %2\n\ts_nop 0\n\tglobal_load_lds_dwordx4 %1, off nt\n\ts_mov_b32 m0, %0"
               : "=&s"(keep) : "v"(gsrc), "s"(lds_dst) : "memory");
}
template <int N> struct ic { static constexpr int value = N; };
typedef __amdgpu_buffer_rsrc_t rsrc_t;
__device__ __forceinline__ v4u ldstream(rsrc_t r, int voff, int soff) { return __builtin_amdgcn_raw_buffer_load_b128(r, voff, soff, AUX_NT); }
__device__ __forceinline__ rsrc_t mkrsrc(const void* p, int bytes) { return __builtin_amdgcn_make_buffer_rsrc((void*)p, 0, bytes, 0x00020000); }
__device__ __forceinline__ void publish(rsrc_t rs, int off, unsigned a, unsigned b, unsigned T) {
  v4u v = {a, T, b, T};
  __builtin_amdgcn_raw_buffer_store_b128(v, rs, off, 0, AUX_SC1);
}
template <int N, int SLP> __device__ __forceinline__ void gather(rsrc_t rs, const int (&off)[N], v4u (&v)[N], unsigned T) {
#pragma unroll
  for (int k = 0; k < N; ++k) v[k] = __builtin_amdgcn_raw_buffer_load_b128(rs, off[k], 0, AUX_SC1);
  for (unsigned spins = 0; spins < SPIN_CAP; ++spins) {
    bool ok = true;
#pragma unroll
    for (int k = 0; k < N; ++k) ok &= (v[k].y == T) & (v[k].w == T);
    if (__all(ok)) break;
    __builtin_amdgcn_s_sleep(SLP);
    asm volatile("" ::: "memory");
#pragma unroll
    for (int k = 0; k < N; ++k)
      if ((v[k].y != T) | (v[k].w != T)) v[k] = __builtin_amdgcn_raw_buffer_load_b128(rs, off[k], 0, AUX_SC1);
  }
}
__device__ __forceinline__ void lds_wait(volatile LAS unsigned* f, unsigned want) {
  for (unsigned spins = 0; spins < (1u << 22); ++spins) {
    if (__builtin_amdgcn_readfirstlane(*f) >= want) break;
    __builtin_amdgcn_s_sleep(1);
  }
  asm volatile("" ::: "memory");
}
__device__ __forceinline__ void lds_spin(volatile LAS unsigned* f, unsigned want) {
  for (unsigned spins = 0; spins < (1u << 24); ++spins)
    if (__builtin_amdgcn_readfirstlane(*f) >= want) break;
  asm volatile("" ::: "memory");
}
__device__ __forceinline__ unsigned now() { return (unsigned)__builtin_amdgcn_s_memrealtime(); }
__device__ __forceinline__ void nap_until(unsigned t0, unsigned ticks) {
  for (int i = 0; i < 4096 && now() - t0 < ticks; ++i) __builtin_amdgcn_s_sleep(4);
}
__device__ __forceinline__ void lds_inc(volatile LAS unsigned* f) {
  __hip_atomic_fetch_add((LAS unsigned*)f, 1u, __ATOMIC_RELAXED, __HIP_MEMORY_SCOPE_WORKGROUP);
}
__device__ __forceinline__ float ln_rsd(float var) { return rcp_nr(rbf(__builtin_amdgcn_sqrtf(rbf(rbf(var) + 1e-5f)))); }

__global__ void __launch_bounds__(NTHR, 3) fused_block(Params p) {
  extern __shared__ __attribute__((aligned(16))) unsigned char smem[];
  LAS unsigned char* L = (LAS unsigned char*)smem;
  volatile LAS unsigned* flags = (volatile LAS unsigned*)(L + L_FLAG);
  const int bid = blockIdx.x, tid = threadIdx.x, lane = tid & 63;
  const int wave = __builtin_amdgcn_readfirstlane(tid >> 6);
  const int xcd = bid & 7, li = bid >> 3;
  const int cu = xcd * 32 + li;
  const int h = cu >> 4, b = (cu >> 3) & 1, c = cu & 7, kh = c >> 2, dq = c & 3, bh = b * 16 + h;
  const bool q3 = (li & 3) == 3;
  const int jn = (li >> 2) * 3 + (li & 3);
  const int mu = q3 ? 136 + (li >> 2) * 8 + xcd : jn * 8 + xcd;
  const int mb = mu >> 2, mpart = mu & 3;
  const bool is_mod = q3 ? li < 16 : jn < 17, is_ffn = !q3 && jn >= 17, is_red = !q3 && jn < 14;
  const int fidx = xcd * 7 + jn - 17;
  const int rb = jn & 1, rchunk = xcd * 7 + (jn >> 1);
  asm volatile("" ::"s"(p.x), "s"(p.kc), "s"(p.vc), "s"(p.wq), "s"(p.wo), "s"(p.c), "s"(p.wmod), "s"(p.ws));
  const rsrc_t rs = mkrsrc(p.ws, WS_END);
  const unsigned tstart = now();
  unsigned e0 = 0u;
  if (wave == 0) e0 = __builtin_amdgcn_raw_buffer_load_b32(rs, WS_EPOCH, 0, AUX_SC1);
  if (tid < 16) flags[tid] = 0u;
  unsigned T;
  v4u bw[8];
  float kp[8];
  const int t0 = wave < 4 ? 16 + wave : wave - 4, t1 = wave < 4 ? 20 + wave : wave + 4, t2 = wave < 4 ? 24 + wave : 0;
  const int w0 = wave < 4 ? 24 : wave - 4, wst = wave < 4 ? 1 : 8;
  const int tr_row = 8 * (lane >> 4) + ((lane >> 2) & 3), tr_col = (lane & 3) * 8, g16 = lane >> 4;

  if (wave >= 4) {
    const int sw = wave - 4;
    const rsrc_t rk = mkrsrc(p.kc, 64 << 20), rv = mkrsrc(p.vc, 64 << 20);
    const int koff = (bh * SK + c * 512) * 512 + sw * 1024 + lane * 16;
    const int voff = (bh * SK + kh * 2048 + sw * 8 + (lane >> 3)) * 512 + dq * 128 + (lane & 7) * 16;
    v4u kb[32], vb[32];
    const bool bfirst = is_mod && mu < 56;
    if (bfirst) bar();
#pragma unroll
    for (int i = 0; i < 32; ++i) {
      if (i == KDEPTH && !bfirst) bar();
      if (i >= KDEPTH) waitvm<KDEPTH - 1>();
      kb[i] = ldstream(rk, koff, i * 8192);
      __builtin_amdgcn_sched_barrier(0);
    }
    if (sw == 0 && lane == 0) flags[F_KI] = 1u;
    lds_wait(flags + F_U, 3u);
    const GAS unsigned char* vsrc = (const GAS unsigned char*)p.vc + voff;
    const unsigned vdst = (unsigned)(size_t)(L + L_VE) + sw * 1024;
    int ev = 0;
    while (ev < VEARLY && __builtin_amdgcn_readfirstlane(flags[F_Q]) == 0u) {
      waitvm<VDEPTH - 1>();
      glds16_nt(vsrc + ev * 32768, vdst + ev * 8192);
      ++ev;
    }
    lds_wait(flags + F_Q, 1u);
    const v4u qv = *(const LAS v4u*)(L + L_Q + (lane & 31) * 16);
    float s2[16];
#pragma unroll
    for (int m = 0; m < 16; ++m) {
      float a = 0.f, bb = 0.f;
#pragma unroll
      for (int j = 0; j < 4; ++j) { a = dot2(kb[2 * m][j], qv[j], a); bb = dot2(kb[2 * m + 1][j], qv[j], bb); }
      s2[m] = tr16(a, bb);
      if ((m & 3) == 3) {
        __builtin_amdgcn_sched_barrier(0);
        if (ev < VEARLY) { waitvm<VDEPTH - 1>(); glds16_nt(vsrc + ev * 32768, vdst + ev * 8192); ++ev; }
        waitvm<VDEPTH - 1>();
        vb[VEARLY + (m >> 2)] = ldstream(rv, voff, (VEARLY + (m >> 2)) * 32768);
        __builtin_amdgcn_sched_barrier(0);
      }
    }
    float s4[8], s8[4], s16[2];
#pragma unroll
    for (int u = 0; u < 8; ++u) s4[u] = trd<ROR8>(s2[2 * u], s2[2 * u + 1], lane & 8);
#pragma unroll
    for (int u = 0; u < 4; ++u) s8[u] = trd<XOR7>(s4[u], s4[u + 4], lane & 4);
#pragma unroll
    for (int u = 0; u < 2; ++u) s16[u] = trd<XOR2>(s8[u], s8[u + 2], lane & 2);
    const float sc = trd<XOR1>(s16[0], s16[1], lane & 1);
    T = __builtin_amdgcn_readfirstlane(flags[F_T]);
    {
      const unsigned pr = pack2(sc, dpp<XOR1>(sc));
      const unsigned pr2 = dppu<XOR2>(pr);
      if ((lane & 3) == 0) publish(rs, WS_SC + bh * 16384 + ((c * 8 + sw) * 16 + (lane >> 2)) * 16, pr, pr2, T);
      if (lane == 0) lds_inc(flags + F_SC);
    }
    while (ev < VEARLY) { waitvm<VDEPTH - 1>(); glds16_nt(vsrc + ev * 32768, vdst + ev * 8192); ++ev; }
    float acc[8];
#pragma unroll
    for (int j = 0; j < 8; ++j) acc[j] = 0.f;
    auto pv = [&](auto m0_, auto m1_) {
      constexpr int M0 = decltype(m0_)::value, M1 = decltype(m1_)::value;
      v4u pp[4];
#pragma unroll
      for (int u = M0 >> 2; u <= (M1 - 1) >> 2; ++u) pp[u] = *(const LAS v4u*)(L + L_P + (sw * 8 + (lane >> 3)) * 64 + u * 16);
#pragma unroll
      for (int m = M0; m < M1; ++m) {
        const unsigned pw = pp[m >> 2][m & 3];
        const v4u va = 2 * m < VEARLY ? *(const LAS v4u*)(L + L_VE + (2 * m * 8 + sw) * 1024 + lane * 16) : vb[2 * m];
        const v4u vc2 = 2 * m + 1 < VEARLY ? *(const LAS v4u*)(L + L_VE + ((2 * m + 1) * 8 + sw) * 1024 + lane * 16) : vb[2 * m + 1];
#pragma unroll
        for (int j = 0; j < 4; ++j) {
          const unsigned a = va[j], bb = vc2[j];
          acc[2 * j] = dot2(__builtin_amdgcn_perm(bb, a, 0x05040100u), pw, acc[2 * j]);
          acc[2 * j + 1] = dot2(__builtin_amdgcn_perm(bb, a, 0x07060302u), pw, acc[2 * j + 1]);
        }
      }
#pragma unroll
      for (int j = 0; j < 8; ++j) asm volatile("" : "+v"(acc[j]));
    };
    constexpr int PA = (PCHK - VDEPTH + 1) / 2, PB = (31 - VDEPTH + 1) / 2;
    bool early = false;
#pragma unroll
    for (int i = 4 + VEARLY; i < 32; ++i) {
      __builtin_amdgcn_sched_barrier(0);
      waitvm<VDEPTH - 1>();
      vb[i] = ldstream(rv, voff, i * 32768);
      __builtin_amdgcn_sched_barrier(0);
      if (i == PCHK) {
        early = __builtin_amdgcn_readfirstlane(flags[F_P]) != 0u;
        asm volatile("" ::: "memory");
        if (early) pv(ic<0>{}, ic<PA>{});
      }
    }
    if (!early) { lds_wait(flags + F_P, 1u); pv(ic<0>{}, ic<PA>{}); }
    pv(ic<PA>{}, ic<PB>{});
    lds_wait(flags + F_WO, 1u);
#pragma unroll
    for (int s = 0; s < 2; ++s) {
      bw[s] = bfrag(L + L_WO + (32 * s + tr_row) * (D * 2) + w0 * 32 + tr_col, D * 2);
      bw[2 + s] = bfrag(L + L_WO + (32 * s + tr_row) * (D * 2) + (w0 + 8) * 32 + tr_col, D * 2);
      bw[4 + s] = bfrag(L + L_WO + (32 * s + tr_row) * (D * 2) + (w0 + 16) * 32 + tr_col, D * 2);
    }
    pv(ic<PB>{}, ic<16>{});
    float a4[4], a2[2];
#pragma unroll
    for (int u = 0; u < 4; ++u) a4[u] = tr32(acc[u], acc[u + 4]);
#pragma unroll
    for (int u = 0; u < 2; ++u) a2[u] = tr16(a4[u], a4[u + 2]);
    const float o1 = trd<ROR8>(a2[0], a2[1], lane & 8);
    const int dim = (lane & 7) * 8 + ((lane >> 5) & 1) * 4 + ((lane >> 4) & 1) * 2 + ((lane >> 3) & 1);
    const float oh = rbf(o1);
    const unsigned hl = pack2(oh, o1 - oh);
    *(LAS u16*)(L + L_OA + sw * 144 + dim * 2) = (u16)hl;
    *(LAS u16*)(L + L_OA + (8 + sw) * 144 + dim * 2) = (u16)(hl >> 16);
    waitlds();
    if (lane == 0) lds_inc(flags + F_O);
  } else {
    const int a = wave;
#pragma unroll
    for (int j = 0; j < 8; ++j) kp[j] = 0.f;
    const rsrc_t rx = mkrsrc(p.x, 2 * D * 2);
    const int l56 = lane < 56 ? lane : 55, wsmod = WS_MOD + xcd * MOD_REP;
    v4u xa, xb; unsigned cv0, cv1, xd0, xd1;
    const bool qhelp = !q3 && jn >= 16;
    const bool do_q = a == 0 ? !(q3) : (a == 3 && qhelp);
    const int qcu = a == 0 ? cu : xcd * 32 + (jn < 17 ? 7 : jn - 17) * 4 + 3;
    const int qh = qcu >> 4, rq = (qcu & 15) >> 2, cbq = qcu & 3;
    const int l_wq = a == 0 ? L_WQ : L_GU + 16384, l_h1 = a == 0 ? L_H1 : L_H1 + 512;
    const int qk0 = rq < 2 ? rq * 128 : 64 + rq * 96, qnr = rq < 2 ? 128 : 96;
    const int qr = 2 * lane < qnr ? 2 * lane : qnr - 2;
    const int mk0 = mpart < 2 ? mpart * 128 : 64 + mpart * 96, mnr = mpart < 2 ? 128 : 96;
    if (is_mod && a == 2) {
      const rsrc_t rc = mkrsrc(p.c, 2 * D * 2);
      const int mr = 2 * lane < mnr ? 2 * lane : mnr - 2;
      cv0 = __builtin_amdgcn_raw_buffer_load_b32(rc, (mk0 + mr) * 2, 0, 0);
      cv1 = __builtin_amdgcn_raw_buffer_load_b32(rc, (D + mk0 + mr) * 2, 0, 0);
      const GAS unsigned char* src = (const GAS unsigned char*)p.wmod + (size_t)(mk0 + (lane >> 3)) * (6 * D * 2) + mb * 128 + (lane & 7) * 16;
#pragma unroll
      for (int i = 0; i < 16; ++i)
        if (i < 12 || mpart < 2)
          __builtin_amdgcn_global_load_lds((const GAS unsigned*)(src + (size_t)i * 8 * (6 * D * 2)), (LAS unsigned*)(L + L_WM + i * 1024), 16, 0, AUX_NT);
    }
    if (do_q) {
      xa = __builtin_amdgcn_raw_buffer_load_b128(rx, l56 * 16, 0, 0);
      xb = __builtin_amdgcn_raw_buffer_load_b128(rx, D * 2 + l56 * 16, 0, 0);
      xd0 = __builtin_amdgcn_raw_buffer_load_b32(rx, (qk0 + qr) * 2, 0, 0);
      xd1 = __builtin_amdgcn_raw_buffer_load_b32(rx, (D + qk0 + qr) * 2, 0, 0);
      const GAS unsigned char* src = (const GAS unsigned char*)p.wq + (size_t)(qk0 + (lane >> 3)) * (NH * DH * 2) + (qh * 256 + cbq * 64) * 2 + (lane & 7) * 16;
#pragma unroll
      for (int i = 0; i < 16; ++i)
        if (i < 12 || rq < 2)
          __builtin_amdgcn_global_load_lds((const GAS unsigned*)(src + (size_t)i * 8 * (NH * DH * 2)), (LAS unsigned*)(L + l_wq + i * 1024), 16, 0, AUX_NT);
    }
    bar();
    if (a == 0) {
      T = __builtin_amdgcn_readfirstlane(e0) + 1u;
      if (lane == 0) { flags[F_T] = T; waitlds(); flags[F_TV] = 1u; }
    } else {
      lds_wait(flags + F_TV, 1u);
      T = __builtin_amdgcn_readfirstlane(flags[F_T]);
    }
    if (!is_mod && a == 2 && lane == 0) lds_inc(flags + F_U);
    if (is_mod && a == 2) {
      {
        const float c00 = lo16(cv0), c01 = hi16(cv0), c10 = lo16(cv1), c11 = hi16(cv1);
        const float s00 = c00 / (1.f + __expf(-c00)), s01 = c01 / (1.f + __expf(-c01));
        const float s10 = c10 / (1.f + __expf(-c10)), s11 = c11 / (1.f + __expf(-c11));
        const bool live = 2 * lane < mnr;
        *(LAS unsigned*)(L + L_H2 + lane * 4) = live ? pack2(s00, s01) : 0u;
        *(LAS unsigned*)(L + L_H2 + 256 + lane * 4) = live ? pack2(s10, s11) : 0u;
      }
      waitvm<0>();
      v4f d[4];
#pragma unroll
      for (int t = 0; t < 4; ++t) d[t] = v4f{0.f, 0.f, 0.f, 0.f};
      LAS unsigned char* mq = L + L_WM + tr_row * 128 + tr_col;
#pragma unroll
      for (int s = 0; s < 4; ++s) {
        const v4u aa = *(const LAS v4u*)(L + L_H2 + (lane & 1) * 256 + s * 64 + g16 * 16);
#pragma unroll
        for (int t = 0; t < 4; ++t) d[t] = mfma(aa, bfrag(mq + (s < 3 || mpart < 2 ? s : 0) * 4096 + t * 32, 128), d[t]);
      }
      waitlds();
      if (lane == 0) lds_inc(flags + F_U);
      float m0 = g16 == 0 ? d[0][0] : (g16 == 1 ? d[1][0] : (g16 == 2 ? d[2][0] : d[3][0]));
      float m1 = g16 == 0 ? d[0][1] : (g16 == 1 ? d[1][1] : (g16 == 2 ? d[2][1] : d[3][1]));
      if (mpart != 0) {
        publish(rs, WS_MP + ((mb * 3 + mpart - 1) * 64 + lane) * 16, __float_as_uint(m0), __float_as_uint(m1), T);
      } else {
        int off[3]; v4u g[3];
#pragma unroll
        for (int k = 0; k < 3; ++k) off[k] = WS_MP + ((mb * 3 + k) * 64 + lane) * 16;
        nap_until(now(), G_MP);
        gather<3, 8>(rs, off, g, T);
        m0 = (m0 + __uint_as_float(g[0].x)) + (__uint_as_float(g[1].x) + __uint_as_float(g[2].x));
        m1 = (m1 + __uint_as_float(g[0].z)) + (__uint_as_float(g[1].z) + __uint_as_float(g[2].z));
        const unsigned gg = pack2(m0, m1), gn = dppu<XOR1>(gg);
        const unsigned lo_ = (lane & 1) ? gn : gg, hi_ = (lane & 1) ? gg : gn;
#pragma unroll
        for (int k = 0; k < NREP / 2; ++k) publish(rs, WS_MOD + (2 * k + (lane & 1)) * MOD_REP + (mb * 32 + (lane >> 1)) * 16, lo_, hi_, T);
      }
    }
    if ((a == 0 || a == 3) && !do_q && lane == 0) lds_inc(flags + F_U);
    if (do_q) {
      waitvm<0>();
      v4u bq[16];
      {
        LAS unsigned char* qq = L + l_wq + tr_row * 128 + tr_col;
#pragma unroll
        for (int s = 0; s < 4; ++s)
#pragma unroll
          for (int t = 0; t < 4; ++t) bq[s * 4 + t] = bfrag(qq + (s < 3 || rq < 2 ? s : 0) * 4096 + t * 32, 128);
      }
      waitlds();
      if (lane == 0) lds_inc(flags + F_U);
      float sm0 = 0.f, sm1 = 0.f;
#pragma unroll
      for (int j = 0; j < 4; ++j) { sm0 += lo16(xa[j]) + hi16(xa[j]); sm1 += lo16(xb[j]) + hi16(xb[j]); }
      if (lane >= 56) { sm0 = 0.f; sm1 = 0.f; }
      const float mean0 = wave_sum(sm0) * (1.f / D), mean1 = wave_sum(sm1) * (1.f / D);
      float v0 = 0.f, v1 = 0.f;
#pragma unroll
      for (int j = 0; j < 4; ++j) {
        float d = lo16(xa[j]) - mean0; v0 = fmaf(d, d, v0); d = hi16(xa[j]) - mean0; v0 = fmaf(d, d, v0);
        d = lo16(xb[j]) - mean1; v1 = fmaf(d, d, v1); d = hi16(xb[j]) - mean1; v1 = fmaf(d, d, v1);
      }
      if (lane >= 56) { v0 = 0.f; v1 = 0.f; }
      const float rs0 = ln_rsd(wave_sum(v0) * (1.f / D)), rs1 = ln_rsd(wave_sum(v1) * (1.f / D));
      const float mb0 = rbf(mean0), mb1 = rbf(mean1);
      const float n00 = rbf(rbf(lo16(xd0) - mb0) * rs0), n01 = rbf(rbf(hi16(xd0) - mb0) * rs0);
      const float n10 = rbf(rbf(lo16(xd1) - mb1) * rs1), n11 = rbf(rbf(hi16(xd1) - mb1) * rs1);
      {
        int off[2] = {wsmod + ((qk0 + qr) >> 1) * 16, wsmod + (224 + ((qk0 + qr) >> 1)) * 16}; v4u g[2];
        nap_until(tstart, G_MOD);
        gather<2, PSLP>(rs, off, g, T);
        const float h00 = rbf(n00 * rbf(1.f + lo16(g[1].x))) + lo16(g[0].x), h10 = rbf(n10 * rbf(1.f + hi16(g[1].x))) + hi16(g[0].x);
        const float h01 = rbf(n01 * rbf(1.f + lo16(g[1].z))) + lo16(g[0].z), h11 = rbf(n11 * rbf(1.f + hi16(g[1].z))) + hi16(g[0].z);
        const bool live = 2 * lane < qnr;
        *(LAS unsigned*)(L + l_h1 + lane * 4) = live ? pack2(h00, h01) : 0u;
        *(LAS unsigned*)(L + l_h1 + 256 + lane * 4) = live ? pack2(h10, h11) : 0u;
      }
      {
        v4f d[4];
#pragma unroll
        for (int t = 0; t < 4; ++t) d[t] = v4f{0.f, 0.f, 0.f, 0.f};
#pragma unroll
        for (int s = 0; s < 4; ++s) {
          const v4u aa = *(const LAS v4u*)(L + l_h1 + (lane & 1) * 256 + s * 64 + g16 * 16);
#pragma unroll
          for (int t = 0; t < 4; ++t) d[t] = mfma(aa, bq[s * 4 + t], d[t]);
        }
        const float q0 = g16 == 0 ? d[0][0] : (g16 == 1 ? d[1][0] : (g16 == 2 ? d[2][0] : d[3][0]));
        const float q1 = g16 == 0 ? d[0][1] : (g16 == 1 ? d[1][1] : (g16 == 2 ? d[2][1] : d[3][1]));
        const bool odd = lane & 1;
        const float rcv = dpp<XOR1>(odd ? q0 : q1);
        publish(rs, WS_Q + ((((lane & 1) * 16 + qh) * 4 + rq) * 128 + cbq * 32 + (lane >> 1)) * 16,
                __float_as_uint(odd ? rcv : q0), __float_as_uint(odd ? q1 : rcv), T);
      }
    }
    if (a == 0) {
      {
        int off[8]; v4u g[8];
#pragma unroll
        for (int k = 0; k < 8; ++k) off[k] = WS_Q + bh * 8192 + (k * 64 + lane) * 16;
        if (do_q) nap_until(now(), G_Q); else nap_until(tstart, G_Q3);
        gather<8, PSLP>(rs, off, g, T);
#pragma unroll
        for (int u = 0; u < 2; ++u) {
          const float qa = (__uint_as_float(g[u].x) + __uint_as_float(g[2 + u].x)) + (__uint_as_float(g[4 + u].x) + __uint_as_float(g[6 + u].x));
          const float qb = (__uint_as_float(g[u].z) + __uint_as_float(g[2 + u].z)) + (__uint_as_float(g[4 + u].z) + __uint_as_float(g[6 + u].z));
          *(LAS unsigned*)(L + L_Q + (u * 64 + lane) * 4) = pack2(qa, qb);
        }
        waitlds();
        if (lane == 0) flags[F_Q] = 1u;
      }
      lds_wait(flags + F_SC, 8u);
      {
        int off[16]; v4u g[16];
#pragma unroll
        for (int k = 0; k < 16; ++k) off[k] = WS_SC + bh * 16384 + (((k ^ (kh * 8)) * 64) + lane) * 16;
        nap_until(now(), G_SC);
        gather<16, SCSLP>(rs, off, g, T);
        float m = -INFINITY;
#pragma unroll
        for (int k = 0; k < 16; ++k) m = fmaxf(fmaxf(fmaxf(lo16(g[k].x), hi16(g[k].x)), fmaxf(lo16(g[k].z), hi16(g[k].z))), m);
        m = wave_max(m);
        const float cs = 0.0625f * 1.4426950408889634f;
        const float mc = m * cs;
        float e[32]; float l = 0.f;
#pragma unroll
        for (int k = 0; k < 16; ++k) {
          const float e0_ = __builtin_amdgcn_exp2f(fmaf(lo16(g[k].x), cs, -mc)), e1_ = __builtin_amdgcn_exp2f(fmaf(hi16(g[k].x), cs, -mc));
          const float e2_ = __builtin_amdgcn_exp2f(fmaf(lo16(g[k].z), cs, -mc)), e3_ = __builtin_amdgcn_exp2f(fmaf(hi16(g[k].z), cs, -mc));
          l += (e0_ + e1_) + (e2_ + e3_);
          if (k < 8) { e[4 * k] = e0_; e[4 * k + 1] = e1_; e[4 * k + 2] = e2_; e[4 * k + 3] = e3_; }
        }
        l = wave_sum(l);
        const float rl = rcp_nr(l);
        const int t = lane & 15;
        const int half = t >> 3, j0 = (t >> 2) & 1, j1 = (t >> 1) & 1, j4 = t & 1;
#pragma unroll
        for (int k = 0; k < 8; ++k) {
          const int cq = k >> 1;
          const int wp = (k & 1) * 4 + (lane >> 4);
          const int wc = (j1 << 2) | (j0 << 1) | (wp >> 2), gg = ((wp & 3) << 1) | half, mm = (cq << 2) | (j4 << 1);
          *(LAS v2u*)(L + L_P + ((wc * 8 + gg) * 16 + mm) * 4) = v2u{pack2(e[4 * k] * rl, e[4 * k + 1] * rl), pack2(e[4 * k + 2] * rl, e[4 * k + 3] * rl)};
        }
        waitlds();
        if (lane == 0) flags[F_P] = 1u;
      }
    } else if (a == 1) {
      {
        unsigned long long pc; int len;
        asm volatile("s_getpc_b64 %0\n.Lfb_here:\n\ts_mov_b32 %1, .Lfb_end-.Lfb_here" : "=s"(pc), "=s"(len));
        const int o = ((li & 3) * 64 + lane) * 128;
        const unsigned t_ = *(const GAS unsigned*)((pc & ~3ull) + (unsigned)(o < len ? o : 0));
        asm volatile("" ::"v"(t_));
      }
      lds_wait(flags + F_KI, 1u);
      const GAS unsigned char* src = (const GAS unsigned char*)p.wo + (size_t)(h * 256 + dq * 64) * (D * 2) + lane * 16;
#pragma unroll
      for (int i = 0; i < 56; ++i) {
        if (i >= WDEPTH) waitvm<WDEPTH - 1>();
        __builtin_amdgcn_global_load_lds((const GAS unsigned*)(src + i * 1024), (LAS unsigned*)(L + L_WO + i * 1024), 16, 0, WO_POL);
      }
      waitvm<0>();
      if (lane == 0) flags[F_WO] = 1u;
    }
    if (a == 3) {
      if (is_red) {
        const unsigned xr = __builtin_amdgcn_raw_buffer_load_b32(rx, (rb * D + rchunk * 8 + (lane & 3) * 2) * 2, 0, 0);
        int off[2] = {wsmod + (D + rchunk * 4 + (lane & 3)) * 16, wsmod + (5 * D / 2 + rchunk * 4 + (lane & 3)) * 16}; v4u g[2];
        lds_wait(flags + F_KI, 1u);
        gather<2, 32>(rs, off, g, T);
        kp[0] = rb ? hi16(g[0].x) : lo16(g[0].x); kp[1] = rb ? hi16(g[0].z) : lo16(g[0].z);
        kp[2] = rb ? hi16(g[1].x) : lo16(g[1].x); kp[3] = rb ? hi16(g[1].z) : lo16(g[1].z);
        kp[4] = lo16(xr); kp[5] = hi16(xr);
      }
    }
    if (is_ffn) {
      const int col2 = ((a & 1) * 56 + l56) * 2;
      int off[4] = {wsmod + (2 * D + col2) * 16, wsmod + (2 * D + col2 + 1) * 16, wsmod + (3 * D / 2 + col2) * 16, wsmod + (3 * D / 2 + col2 + 1) * 16};
      v4u g[4];
      lds_wait(flags + F_KI, 1u);
      gather<4, 32>(rs, off, g, T);
      const bool hb = a >> 1;
      kp[0] = rbf(1.f + (hb ? hi16(g[0].x) : lo16(g[0].x))); kp[1] = rbf(1.f + (hb ? hi16(g[0].z) : lo16(g[0].z)));
      kp[2] = rbf(1.f + (hb ? hi16(g[1].x) : lo16(g[1].x))); kp[3] = rbf(1.f + (hb ? hi16(g[1].z) : lo16(g[1].z)));
      kp[4] = hb ? hi16(g[2].x) : lo16(g[2].x); kp[5] = hb ? hi16(g[2].z) : lo16(g[2].z);
      kp[6] = hb ? hi16(g[3].x) : lo16(g[3].x); kp[7] = hb ? hi16(g[3].z) : lo16(g[3].z);
    }
    if (a == 0) {
      lds_wait(flags + F_WO, 1u);
#pragma unroll
      for (int s = 0; s < 8; ++s) bw[s] = bfrag(L + L_WO + (32 * (s & 1) + tr_row) * (D * 2) + (24 + (s >> 1)) * 32 + tr_col, D * 2);
    }
  }

  const int n16 = lane & 15;
  const int tsel = g16 == 0 ? t0 : (g16 == 2 ? t1 : t2);
  if (wave == 0 || wave >= 4) {
    const int n = (w0 + g16 * wst) * 16 + n16;
    int poff = WS_P1 + (((b * 56 + (n >> 3)) * 128 + h * 8 + c) * 4 + ((n >> 1) & 3)) * 16;
    asm volatile("" : "+v"(poff));
    lds_spin(flags + F_O, 8u);
    const v4u a0 = *(const LAS v4u*)(L + L_OA + n16 * 144 + g16 * 16), a1 = *(const LAS v4u*)(L + L_OA + n16 * 144 + 64 + g16 * 16);
    const v4f z = {0.f, 0.f, 0.f, 0.f};
    const v4f d0 = mfma(a1, bw[1], mfma(a0, bw[0], z)), d1 = mfma(a1, bw[3], mfma(a0, bw[2], z));
    const v4f d2 = mfma(a1, bw[5], mfma(a0, bw[4], z));
    v4f d3 = z;
    if (wave == 0) d3 = mfma(a1, bw[7], mfma(a0, bw[6], z));
    const float u1 = tr32((d0[0] + d0[1]) + (d0[2] + d0[3]), (d2[0] + d2[1]) + (d2[2] + d2[3]));
    const float u2 = tr32((d1[0] + d1[1]) + (d1[2] + d1[3]), (d3[0] + d3[1]) + (d3[2] + d3[3]));
    const float v = tr16(u1, u2);
    const float vn = dpp<XOR1>(v);
    if ((lane & 1) == 0 && (g16 < 3 || wave == 0)) publish(rs, poff, __float_as_uint(v), __float_as_uint(vn), T);
    if (lane == 0) lds_inc(flags + F_PUB);
  }
  if (!is_ffn && wave >= 4 && wave < 8) {
    const int slot = (q3 ? 17 + (li >> 2) : jn) * 4 + wave - 4;
    const int ln = slot * 64 + lane;
    const GAS unsigned char* a_ = ln < 3584 ? (const GAS unsigned char*)((ln & 4) ? p.wu : p.wg) + (size_t)(ln >> 3) * (F * 2) + ((xcd * 448) & ~127) + (ln & 3) * 128
                                            : (const GAS unsigned char*)p.wd + (size_t)xcd * 7 * 32 * (D * 2) + (size_t)(ln < 5152 ? ln - 3584 : 0) * 128;
    const unsigned t_ = *(const GAS unsigned*)a_;
    asm volatile("" ::"v"(t_));
  }
  if (is_red) {
    if (wave == 3) {
      float x20, x21;
      lds_wait(flags + F_PUB, 9u);
      {
        int off[8]; v4u g[8];
#pragma unroll
        for (int k = 0; k < 8; ++k) off[k] = WS_P1 + (rb * 56 + rchunk) * 8192 + (k * 64 + lane) * 16;
        gather<8, 1>(rs, off, g, T);
        float s0 = 0.f, s1 = 0.f;
#pragma unroll
        for (int k = 0; k < 8; k += 2) { s0 += __uint_as_float(g[k].x) + __uint_as_float(g[k + 1].x); s1 += __uint_as_float(g[k].z) + __uint_as_float(g[k + 1].z); }
        s0 += dpp<ROR4>(s0); s1 += dpp<ROR4>(s1); s0 += dpp<ROR8>(s0); s1 += dpp<ROR8>(s1);
        s0 = sum16(s0); s1 = sum16(s1); s0 = sum32(s0); s1 = sum32(s1);
        x20 = rbf(kp[4] + rbf(kp[0] * rbf(s0))); x21 = rbf(kp[5] + rbf(kp[1] * rbf(s1)));
        const unsigned xp = pack2(x20, x21), xq = dppu<XOR1>(xp);
        float sm = x20 + x21; sm += dpp<XOR1>(sm); sm += dpp<XOR2>(sm);
        const float d0 = x20 - sm * 0.125f, d1 = x21 - sm * 0.125f;
        float m2 = fmaf(d0, d0, d1 * d1); m2 += dpp<XOR1>(m2); m2 += dpp<XOR2>(m2);
        const bool stl = (lane & 3) == 1;
        if ((lane & 3) < 3 && lane < 4 * NREP)
          publish(rs, (lane >> 2) * XS_REP + (stl ? WS_ST + (rb * 56 + rchunk) * 16 : WS_X2 + (rb * 112 + rchunk * 2 + ((lane >> 1) & 1)) * 16),
                  stl ? __float_as_uint(sm) : xp, stl ? __float_as_uint(m2) : xq, T);
      }
      {
        int off[4]; v4u g[4];
#pragma unroll
        for (int k = 0; k < 4; ++k) { int pc = k * 64 + lane; pc = pc < 224 ? pc : pc - 32; off[k] = WS_P2 + (rb * 56 + rchunk) * (NF * 64) + pc * 16; }
          gather<4, 1>(rs, off, g, T);
        float s0 = (__uint_as_float(g[0].x) + __uint_as_float(g[1].x)) + (__uint_as_float(g[2].x) + (lane < 32 ? __uint_as_float(g[3].x) : 0.f));
        float s1 = (__uint_as_float(g[0].z) + __uint_as_float(g[1].z)) + (__uint_as_float(g[2].z) + (lane < 32 ? __uint_as_float(g[3].z) : 0.f));
        s0 += dpp<ROR4>(s0); s1 += dpp<ROR4>(s1); s0 += dpp<ROR8>(s0); s1 += dpp<ROR8>(s1);
        s0 = sum16(s0); s1 = sum16(s1); s0 = sum32(s0); s1 = sum32(s1);
        const unsigned ov = pack2(x20 + rbf(kp[2] * rbf(s0)), x21 + rbf(kp[3] * rbf(s1)));
        if (lane < 4) ((GAS unsigned*)p.out)[(rb * D + rchunk * 8) / 2 + lane] = ov;
        if (bid == 0 && lane == 0) __builtin_amdgcn_raw_buffer_store_b32(T, rs, WS_EPOCH, 0, AUX_SC1);
      }
    }
  } else if (is_ffn) {
    {
      if (wave >= 1 && wave < 4) lds_wait(flags + F_PUB, 9u);
      const GAS unsigned char* gsrc = (const GAS unsigned char*)((lane & 4) ? p.wu : p.wg) + (size_t)(lane >> 3) * (F * 2) + fidx * 64 + (lane & 3) * 16;
      const GAS unsigned char* dsrc = (const GAS unsigned char*)p.wd + (size_t)fidx * 32 * (D * 2) + lane * 16;
#pragma unroll
      for (int i = 0; i < 7; ++i) {
        const int u = i * 12 + wave;
        const GAS unsigned char* src = u < 56 ? gsrc + (size_t)u * 8 * (F * 2) : dsrc + (u - 56) * 1024;
        glds16(src, (unsigned)(size_t)(L + L_GU) + u * 1024);
      }
      asm volatile("s_waitcnt vmcnt(2)" ::: "memory");
      bar();
    }
    v4u bd[3];
    if (wave < 4) {
      v4u bg[14];
      {
        LAS unsigned char* gq = L + L_GU + tr_row * 128 + (lane & 2) * 32 + wave * 16 + (lane & 1) * 8;
#pragma unroll
        for (int s = 0; s < 14; ++s) bg[s] = bfrag(gq + s * 32 * 128, 128);
      }
      const int l56 = lane < 56 ? lane : 55, xbt = wave >> 1;
      {
        int off[2] = {WS_X2 + xcd * XS_REP + (wave * 56 + l56) * 16, WS_ST + xcd * XS_REP + (xbt * 56 + l56) * 16}; v4u g[2];
        gather<2, 1>(rs, off, g, T);
        const float cm = __uint_as_float(g[1].x) * 0.125f;
        const float piv = __uint_as_float(__builtin_amdgcn_readfirstlane(__float_as_uint(cm)));
        const float dm = lane < 56 ? cm - piv : 0.f;
        float r0 = dm, r1 = lane < 56 ? fmaf(8.f * dm, dm, __uint_as_float(g[1].z)) : 0.f;
        r0 += dpp<XOR1>(r0); r1 += dpp<XOR1>(r1); r0 += dpp<XOR2>(r0); r1 += dpp<XOR2>(r1);
        r0 += dpp<XOR7>(r0); r1 += dpp<XOR7>(r1); r0 += dpp<ROR8>(r0); r1 += dpp<ROR8>(r1);
        r0 = sum16(r0); r1 = sum16(r1); r0 = sum32(r0); r1 = sum32(r1);
        const float dmean = r0 * (1.f / 56.f);
        const float var = fmaxf(fmaf(-dmean, dmean, r1 * (1.f / D)), 0.f);
        const float rsd = ln_rsd(var), mbf = rbf(piv + dmean);
        const v2f m2_ = {mbf, mbf}, r2_ = {rsd, rsd};
        v2f xa_ = unpk(g[0].x) - m2_, xb_ = unpk(g[0].z) - m2_;
        xa_ = unpk(pack2(xa_.x, xa_.y)) * r2_; xb_ = unpk(pack2(xb_.x, xb_.y)) * r2_;
        xa_ = unpk(pack2(xa_.x, xa_.y)) * v2f{kp[0], kp[1]}; xb_ = unpk(pack2(xb_.x, xb_.y)) * v2f{kp[2], kp[3]};
        xa_ = unpk(pack2(xa_.x, xa_.y)) + v2f{kp[4], kp[5]}; xb_ = unpk(pack2(xb_.x, xb_.y)) + v2f{kp[6], kp[7]};
        *(LAS v2u*)(L + L_H2 + (wave * 56 + l56) * 8) = v2u{pack2(xa_.x, xa_.y), pack2(xb_.x, xb_.y)};
      }
      asm volatile("s_waitcnt vmcnt(0)" ::: "memory");
      bar();
      v4f da[4];
#pragma unroll
      for (int u = 0; u < 4; ++u) da[u] = v4f{0.f, 0.f, 0.f, 0.f};
      v4u af[14];
#pragma unroll
      for (int s = 0; s < 14; ++s) af[s] = *(const LAS v4u*)(L + L_H2 + (lane & 1) * (D * 2) + s * 64 + g16 * 16);
      __builtin_amdgcn_sched_barrier(0);
#pragma unroll
      for (int s = 0; s < 14; ++s) da[s & 3] = mfma(af[s], bg[s], da[s & 3]);
      bd[0] = bfrag(L + L_DN + tr_row * (D * 2) + t0 * 32 + tr_col, D * 2);
      bd[1] = bfrag(L + L_DN + tr_row * (D * 2) + t1 * 32 + tr_col, D * 2);
      bd[2] = bfrag(L + L_DN + tr_row * (D * 2) + t2 * 32 + tr_col, D * 2);
      const float g0 = rbf((da[0][0] + da[1][0]) + (da[2][0] + da[3][0])), g1 = rbf((da[0][1] + da[1][1]) + (da[2][1] + da[3][1]));
      const float u0 = dpp<ROR8>(g0), u1 = dpp<ROR8>(g1);
      const float k1 = -2.f * 0.7978845608028654f * 1.4426950408889634f;
      const float e0_ = __builtin_amdgcn_exp2f(k1 * fmaf(0.044715f * g0 * g0, g0, g0)), e1_ = __builtin_amdgcn_exp2f(k1 * fmaf(0.044715f * g1 * g1, g1, g1));
      const float ge0 = rbf(g0 * __builtin_amdgcn_rcpf(1.f + e0_)), ge1 = rbf(g1 * __builtin_amdgcn_rcpf(1.f + e1_));
      const unsigned av = pack2(ge0 * u0, ge1 * u1);
      if (lane < 8) {
        *(LAS u16*)(L + L_ACT + (wave * 8 + lane) * 2) = (u16)av;
        *(LAS u16*)(L + L_ACT + 64 + (wave * 8 + lane) * 2) = (u16)(av >> 16);
      }
    } else {
      asm volatile("s_waitcnt vmcnt(0)" ::: "memory");
      bar();
      bd[0] = bfrag(L + L_DN + tr_row * (D * 2) + t0 * 32 + tr_col, D * 2);
      bd[1] = bfrag(L + L_DN + tr_row * (D * 2) + t1 * 32 + tr_col, D * 2);
      bd[2] = bfrag(L + L_DN + tr_row * (D * 2) + t2 * 32 + tr_col, D * 2);
    }
    bar();
    {
      const v4u aa = *(const LAS v4u*)(L + L_ACT + (lane & 1) * 64 + g16 * 16);
      const v4f z = {0.f, 0.f, 0.f, 0.f};
      const v4f d0 = mfma(aa, bd[0], z), d1 = mfma(aa, bd[1], z), d2 = mfma(aa, bd[2], z);
      const float v0 = g16 == 0 ? d0[0] : (g16 == 2 ? d1[0] : d2[0]), v1 = g16 == 0 ? d0[1] : (g16 == 2 ? d1[1] : d2[1]);
      const bool odd = lane & 1;
      const float rcv = dpp<XOR1>(odd ? v0 : v1);
      const int n = tsel * 16 + n16;
      if ((g16 & 1) == 0 || (g16 == 1 && wave < 4))
        publish(rs, WS_P2 + ((((lane & 1) * 56 + (n >> 3)) * NF + fidx) * 4 + ((n >> 1) & 3)) * 16,
                __float_as_uint(odd ? rcv : v0), __float_as_uint(odd ? v1 : rcv), T);
    }
  }
  asm volatile(".Lfb_end:");
}
}

extern "C" void kernel_launch(void* const* d_in, const int* in_sizes, int n_in, void* d_out, int out_size, void* d_ws,
                              size_t ws_size, hipStream_t stream) {
  Params p;
  p.x = (const u16*)d_in[0]; p.kc = (const u16*)d_in[1]; p.vc = (const u16*)d_in[2];
  p.wq = (const u16*)d_in[3]; p.wo = (const u16*)d_in[4];
  p.wg = (const u16*)d_in[6]; p.wu = (const u16*)d_in[7]; p.wd = (const u16*)d_in[8];
  p.c = (const u16*)d_in[9]; p.wmod = (const u16*)d_in[10];
  p.out = (u16*)d_out; p.ws = (unsigned char*)d_ws;
  (void)hipFuncSetAttribute((const void*)fused_block, hipFuncAttributeMaxDynamicSharedMemorySize, L_END);
  hipLaunchKernelGGL(fused_block, dim3(256), dim3(NTHR), L_END, stream, p);
}
```
